```python
import torch

def _softmax(x, axis=-1):
    return torch.softmax(x, dim=axis)
def _gelu(x):
    return torch.nn.functional.gelu(x, approximate="tanh")
def _silu(x):
    return torch.nn.functional.silu(x)
def _sigmoid(x):
    return torch.sigmoid(x)
def _layernorm(x, axis=-1, eps=1e-5):
    m = x.mean(dim=axis, keepdim=True)
    v = x.var(dim=axis, keepdim=True, unbiased=False)
    return (x - m) / torch.sqrt(v + eps)
def _scatter_add(tgt, idx, src, axis=0):
    return tgt.clone().index_add_(axis, idx, src)
def _segment_sum(src, seg, n):
    out = torch.zeros((n,) + src.shape[1:], dtype=src.dtype)
    return out.index_add_(0, seg, src)
def _pad(x, pw):
    flat = tuple(v for pair in reversed(pw) for v in pair)
    return torch.nn.functional.pad(x, flat)

kernel_name = 'sequential_preLN__ssm_conv_s4__mixer_token_mlp__prompt'

def setup_inputs():

    _w = (lambda a: (lambda t: t.to(torch.bfloat16) if t.is_floating_point() else t)(torch.as_tensor(a)))
    def _A_setup():
        B, L, D, N = 1, 2048, 512, 128
        A_re = -(lambda _x: torch.exp(torch.as_tensor(_x)))((lambda *s: torch.randn(*s, dtype=torch.bfloat16) * 6.4)(D, N))
        A_im = torch.pi * (lambda *s: torch.randn(*s, dtype=torch.bfloat16) * 0.05)(D, N)
        B_re = (lambda *s: torch.randn(*s, dtype=torch.bfloat16) * 0.1)(D, N)
        B_im = (lambda *s: torch.randn(*s, dtype=torch.bfloat16) * 0.1)(D, N)
        C_re = (lambda *s: torch.randn(*s, dtype=torch.bfloat16) * 0.1)(D, N)
        C_im = (lambda *s: torch.randn(*s, dtype=torch.bfloat16) * 0.1)(D, N)
        log_dt = (lambda *s: torch.randn(*s, dtype=torch.bfloat16) * 0.05)(D) * 0.5 - 4.0
        Dskip = (lambda *s: torch.randn(*s, dtype=torch.bfloat16) * 0.05)(D)
        return {
            "u": _w((lambda *s: torch.randn(*s, dtype=torch.bfloat16) * 0.05)(B, L, D)),
            "A_re": _w(A_re),
            "A_im": _w(A_im),
            "B_re": _w(B_re),
            "B_im": _w(B_im),
            "C_re": _w(C_re),
            "C_im": _w(C_im),
            "log_dt": _w(log_dt),
            "Dskip": _w(Dskip),
        }
    _A_in = _A_setup()

    def _B_setup():
        B, S, D, F = 1, 2048, 512, 1792
        return {
            "x": _w((lambda *s: torch.randn(*s, dtype=torch.bfloat16) * 0.05)(B, S, D)),
            "w1": _w((lambda *s: torch.randn(*s, dtype=torch.bfloat16) * 0.4)(S, F) * (S ** -0.5)),
            "b1": _w((lambda *s: torch.randn(*s, dtype=torch.bfloat16) * 0.1)(F) * 0.02),
            "w2": _w((lambda *s: torch.randn(*s, dtype=torch.bfloat16) * 0.1)(F, S) * (F ** -0.5)),
            "b2": _w((lambda *s: torch.randn(*s, dtype=torch.bfloat16) * 0.1)(S) * 0.02),
        }
    _B_in = _B_setup()

    return {
        "A_u": _A_in["u"],
        "A_A_re": _A_in["A_re"],
        "A_A_im": _A_in["A_im"],
        "A_B_re": _A_in["B_re"],
        "A_B_im": _A_in["B_im"],
        "A_C_re": _A_in["C_re"],
        "A_C_im": _A_in["C_im"],
        "A_log_dt": _A_in["log_dt"],
        "A_Dskip": _A_in["Dskip"],
        "B_x": _B_in["x"],
        "B_w1": _B_in["w1"],
        "B_b1": _B_in["b1"],
        "B_w2": _B_in["w2"],
        "B_b2": _B_in["b2"],
    }

def reference(A_u, A_A_re, A_A_im, A_B_re, A_B_im, A_C_re, A_C_im, A_log_dt, A_Dskip, B_x, B_w1, B_b1, B_w2, B_b2):
    def _A_ref(u, A_re, A_im, B_re, B_im, C_re, C_im, log_dt, Dskip):
        Bsz, L, D = u.shape
        dt = torch.exp(log_dt)[:, None]
        A = A_re + 1j * A_im
        Bc = B_re + 1j * B_im
        Cc = C_re + 1j * C_im
        logdA = dt * A
        dB = dt * Bc
        l = (lambda x, dt: x.to(dt))(torch.arange(L), A_re.dtype)
        P = torch.exp(logdA[:, :, None] * l[None, None, :])
        K = torch.einsum("dn,dnl,dn->dl", Cc, P, dB).real
        L2 = 2 * L
        Kp = _pad(K, ((0, 0), (0, L)))
        up = (lambda x, *d: x.permute(*d))(u, 0, 2, 1)
        up = _pad(up, ((0, 0), (0, 0), (0, L)))
        Kf = (lambda x, axes=(-2,-1): torch.fft.rfft2(x.float(), dim=axes))(Kp[:, None, :], axes=(-2, -1))
        Uf = (lambda x, axes=(-2,-1): torch.fft.rfft2(x.float(), dim=axes))(up[:, :, None, :], axes=(-2, -1))
        Yf = Uf * Kf[None, :, :, :]
        y = (lambda x, s, axes=(-2,-1): torch.fft.irfft2(x, s=s, dim=axes).to(torch.bfloat16))(Yf, (1, L2), axes=(-2, -1))[:, :, 0, :L]
        y = (lambda x, *d: x.permute(*d))(y, 0, 2, 1) + u * Dskip[None, None, :]
        return y

    def _B_ref(x, w1, b1, w2, b2):
        B, S, D = x.shape
        h = _layernorm(x, axis=-1)
        u = torch.einsum("bsd,sf->bdf", h, w1) + b1
        u = _gelu(u)
        y = torch.einsum("bdf,fs->bds", u, w2) + b2
        return (lambda x, *d: x.permute(*d))(y, 0, 2, 1)

    x = A_u
    h = x + _A_ref(_layernorm(x, axis=-1), A_A_re, A_A_im, A_B_re, A_B_im, A_C_re, A_C_im, A_log_dt, A_Dskip)
    h2 = h + _B_ref(_layernorm(h, axis=-1), B_w1, B_b1, B_w2, B_b2)
    return h2
```

```cpp
#include <hip/hip_runtime.h>
#include <hip/hip_bf16.h>
#include <stdint.h>
#define ROUND_L 0
typedef unsigned short u16;
static constexpr int L = 2048, D = 512, N = 128, F = 1792;
__device__ __forceinline__ float bf2f(u16 v) { return __uint_as_float(((unsigned)v) << 16); }
__device__ __forceinline__ u16 f2bf(float f) { unsigned u = __float_as_uint(f); return (u16)((u + 0x7FFF + ((u >> 16) & 1)) >> 16); }
__device__ __forceinline__ float rbf(float f) { return bf2f(f2bf(f)); }

__device__ float block_sum(float v, float* sm) {
  for (int o = 32; o > 0; o >>= 1) v += __shfl_xor(v, o);
  __syncthreads();
  if ((threadIdx.x & 63) == 0) sm[threadIdx.x >> 6] = v;
  __syncthreads();
  float s = 0; for (int i = 0; i < (int)(blockDim.x >> 6); ++i) s += sm[i];
  return s;
}
__global__ void k_ln1(const u16* x, float* u) {
  __shared__ float sm[16];
  int t = blockIdx.x, d = threadIdx.x;
  float v = bf2f(x[t * D + d]);
  float m = block_sum(v, sm) / D;
  float c = v - m;
  float var = block_sum(c * c, sm) / D;
  u[t * D + d] = rbf(c * rsqrtf(var + 1e-5f));
}
__global__ void k_kgen(const u16* Are, const u16* Aim, const u16* Bre, const u16* Bim, const u16* Cre, const u16* Cim, const u16* logdt, float* K) {
  int d = blockIdx.x;
  __shared__ float sr[N], si[N], wr[N], wi[N];
  float dt = rbf(expf(bf2f(logdt[d])));
  if (threadIdx.x < N) {
    int n = threadIdx.x; int i = d * N + n;
    sr[n] = dt * bf2f(Are[i]); si[n] = dt * bf2f(Aim[i]);
    float br = dt * bf2f(Bre[i]), bi = dt * bf2f(Bim[i]);
    float cr = bf2f(Cre[i]), ci = bf2f(Cim[i]);
    wr[n] = cr * br - ci * bi; wi[n] = cr * bi + ci * br;
  }
  __syncthreads();
  for (int l = threadIdx.x; l < L; l += blockDim.x) {
    float lf = ROUND_L ? rbf((float)l) : (float)l;
    float acc = 0;
    for (int n = 0; n < N; ++n) {
      float e = expf(sr[n] * lf); float s, c; sincosf(si[n] * lf, &s, &c);
      acc += e * (wr[n] * c - wi[n] * s);
    }
    K[d * L + l] = acc;
  }
}
__global__ void k_conv(const u16* x, const float* u, const float* K, const u16* Dskip, float* h) {
  int d = blockIdx.x;
  __shared__ float su[L], sk[L];
  for (int l = threadIdx.x; l < L; l += blockDim.x) { su[l] = u[l * D + d]; sk[l] = K[d * L + l]; }
  __syncthreads();
  float ds = bf2f(Dskip[d]);
  for (int l = threadIdx.x; l < L; l += blockDim.x) {
    float acc = 0;
    for (int j = 0; j <= l; ++j) acc += sk[l - j] * su[j];
    h[l * D + d] = rbf(bf2f(x[l * D + d]) + rbf(rbf(acc) + rbf(su[l] * ds)));
  }
}
__global__ void k_ln2(const float* h, float* hn) {
  __shared__ float sm[16];
  int t = blockIdx.x, d = threadIdx.x;
  float v = h[t * D + d];
  float m = block_sum(v, sm) / D;
  float c = v - m;
  float var = block_sum(c * c, sm) / D;
  float r1 = rsqrtf(var + 1e-5f);
  float r2 = rsqrtf(var * r1 * r1 + 1e-5f);
  hn[t * D + d] = rbf(c * r1 * r2);
}
__global__ void k_g1(const float* hn, const u16* w1, const u16* b1, float* G) {
  int f = blockIdx.x * 256 + threadIdx.x, d = blockIdx.y;
  float acc = 0;
  for (int s = 0; s < L; ++s) acc += hn[s * D + d] * bf2f(w1[s * F + f]);
  float v = acc + bf2f(b1[f]);
  float g = 0.5f * v * (1.f + tanhf(0.7978845608028654f * (v + 0.044715f * v * v * v)));
  G[d * F + f] = rbf(g);
}
__global__ void k_g2(const float* G, const u16* w2, const u16* b2, const float* h, u16* out) {
  int s = blockIdx.x * 256 + threadIdx.x, d = blockIdx.y;
  float acc = 0;
  for (int f = 0; f < F; ++f) acc += G[d * F + f] * bf2f(w2[f * L + s]);
  out[s * D + d] = f2bf(h[s * D + d] + acc + bf2f(b2[s]));
}
extern "C" void kernel_launch(void* const* d_in, const int* in_sizes, int n_in, void* d_out, int out_size, void* d_ws, size_t ws_size, hipStream_t stream) {
  const u16* x = (const u16*)d_in[0];
  float* ws = (float*)d_ws;
  float* u = ws; float* K = ws + L * D; float* h = ws + 2 * L * D; float* hn = ws + 3 * L * D; float* G = ws + 4 * L * D;
  k_ln1<<<L, D, 0, stream>>>(x, u);
  k_kgen<<<D, 256, 0, stream>>>((const u16*)d_in[1], (const u16*)d_in[2], (const u16*)d_in[3], (const u16*)d_in[4], (const u16*)d_in[5], (const u16*)d_in[6], (const u16*)d_in[7], K);
  k_conv<<<D, 256, 0, stream>>>(x, u, K, (const u16*)d_in[8], h);
  k_ln2<<<L, D, 0, stream>>>(h, hn);
  k_g1<<<dim3(F / 256, D), 256, 0, stream>>>(hn, (const u16*)d_in[10], (const u16*)d_in[11], G);
  k_g2<<<dim3(L / 256, D), 256, 0, stream>>>(G, (const u16*)d_in[12], (const u16*)d_in[13], h, (u16*)d_out);
}
```

```cpp
#include <hip/hip_runtime.h>
#include <hip/hip_bf16.h>
#include <stdint.h>
#define ROUND_L 1
typedef unsigned short u16;
static constexpr int L = 2048, D = 512, N = 128, F = 1792;
__device__ __forceinline__ float bf2f(u16 v) { return __uint_as_float(((unsigned)v) << 16); }
__device__ __forceinline__ u16 f2bf(float f) { unsigned u = __float_as_uint(f); return (u16)((u + 0x7FFF + ((u >> 16) & 1)) >> 16); }
__device__ __forceinline__ float rbf(float f) { return bf2f(f2bf(f)); }

__device__ float block_sum(float v, float* sm) {
  for (int o = 32; o > 0; o >>= 1) v += __shfl_xor(v, o);
  __syncthreads();
  if ((threadIdx.x & 63) == 0) sm[threadIdx.x >> 6] = v;
  __syncthreads();
  float s = 0; for (int i = 0; i < (int)(blockDim.x >> 6); ++i) s += sm[i];
  return s;
}
__global__ void k_ln1(const u16* x, float* u) {
  __shared__ float sm[16];
  int t = blockIdx.x, d = threadIdx.x;
  float v = bf2f(x[t * D + d]);
  float m = block_sum(v, sm) / D;
  float c = v - m;
  float var = block_sum(c * c, sm) / D;
  u[t * D + d] = rbf(c * rsqrtf(var + 1e-5f));
}
__global__ void k_kgen(const u16* Are, const u16* Aim, const u16* Bre, const u16* Bim, const u16* Cre, const u16* Cim, const u16* logdt, float* K) {
  int d = blockIdx.x;
  __shared__ float sr[N], si[N], wr[N], wi[N];
  float dt = rbf(expf(bf2f(logdt[d])));
  if (threadIdx.x < N) {
    int n = threadIdx.x; int i = d * N + n;
    sr[n] = dt * bf2f(Are[i]); si[n] = dt * bf2f(Aim[i]);
    float br = dt * bf2f(Bre[i]), bi = dt * bf2f(Bim[i]);
    float cr = bf2f(Cre[i]), ci = bf2f(Cim[i]);
    wr[n] = cr * br - ci * bi; wi[n] = cr * bi + ci * br;
  }
  __syncthreads();
  for (int l = threadIdx.x; l < L; l += blockDim.x) {
    float lf = ROUND_L ? rbf((float)l) : (float)l;
    float acc = 0;
    for (int n = 0; n < N; ++n) {
      float e = expf(sr[n] * lf); float s, c; sincosf(si[n] * lf, &s, &c);
      acc += e * (wr[n] * c - wi[n] * s);
    }
    K[d * L + l] = acc;
  }
}
__global__ void k_conv(const u16* x, const float* u, const float* K, const u16* Dskip, float* h) {
  int d = blockIdx.x;
  __shared__ float su[L], sk[L];
  for (int l = threadIdx.x; l < L; l += blockDim.x) { su[l] = u[l * D + d]; sk[l] = K[d * L + l]; }
  __syncthreads();
  float ds = bf2f(Dskip[d]);
  for (int l = threadIdx.x; l < L; l += blockDim.x) {
    float acc = 0;
    for (int j = 0; j <= l; ++j) acc += sk[l - j] * su[j];
    h[l * D + d] = rbf(bf2f(x[l * D + d]) + rbf(rbf(acc) + rbf(su[l] * ds)));
  }
}
__global__ void k_ln2(const float* h, float* hn) {
  __shared__ float sm[16];
  int t = blockIdx.x, d = threadIdx.x;
  float v = h[t * D + d];
  float m = block_sum(v, sm) / D;
  float c = v - m;
  float var = block_sum(c * c, sm) / D;
  float r1 = rsqrtf(var + 1e-5f);
  float r2 = rsqrtf(var * r1 * r1 + 1e-5f);
  hn[t * D + d] = rbf(c * r1 * r2);
}
__global__ void k_g1(const float* hn, const u16* w1, const u16* b1, float* G) {
  int f = blockIdx.x * 256 + threadIdx.x, d = blockIdx.y;
  float acc = 0;
  for (int s = 0; s < L; ++s) acc += hn[s * D + d] * bf2f(w1[s * F + f]);
  float v = acc + bf2f(b1[f]);
  float g = 0.5f * v * (1.f + tanhf(0.7978845608028654f * (v + 0.044715f * v * v * v)));
  G[d * F + f] = rbf(g);
}
__global__ void k_g2(const float* G, const u16* w2, const u16* b2, const float* h, u16* out) {
  int s = blockIdx.x * 256 + threadIdx.x, d = blockIdx.y;
  float acc = 0;
  for (int f = 0; f < F; ++f) acc += G[d * F + f] * bf2f(w2[f * L + s]);
  out[s * D + d] = f2bf(h[s * D + d] + acc + bf2f(b2[s]));
}
#define VAR 0

typedef unsigned v4u __attribute__((ext_vector_type(4)));
typedef unsigned long long u64;
__device__ __forceinline__ u64 rt() { return __builtin_amdgcn_s_memrealtime(); }
__global__ void __launch_bounds__(256, 1) k_probe(const char* x, const char* w1, const char* w2, char* ws) {
  __shared__ char pin[100000];
  const int tid = threadIdx.x, w = blockIdx.x;
  u64 t0 = rt();
  unsigned acc = 0;
  asm volatile("s_waitcnt vmcnt(0)" : "+v"(acc) :: "memory");
  pin[tid] = (char)acc;
  __syncthreads();
  u64 t1 = rt();
  u64 d = t1 - t0; if (d > 2000) d = 2000;
  u64 until = t0 + 200 * d;
  int n = 0;
  while (rt() < until && ++n < 10000000) __builtin_amdgcn_s_sleep(4);
  if (pin[(tid + 1) & 255] == 77 && acc == 0x12345678u) ws[100 << 20] = 1;
}

extern "C" void kernel_launch(void* const* d_in, const int* in_sizes, int n_in, void* d_out, int out_size, void* d_ws, size_t ws_size, hipStream_t stream) {
  const u16* x = (const u16*)d_in[0];
  float* ws = (float*)d_ws;
  float* u = ws; float* K = ws + L * D; float* h = ws + 2 * L * D; float* hn = ws + 3 * L * D; float* G = ws + 4 * L * D;
  k_probe<<<256, 256, 0, stream>>>((const char*)d_in[0], (const char*)d_in[10], (const char*)d_in[12], (char*)d_ws);
  k_ln1<<<L, D, 0, stream>>>(x, u);
  k_kgen<<<D, 256, 0, stream>>>((const u16*)d_in[1], (const u16*)d_in[2], (const u16*)d_in[3], (const u16*)d_in[4], (const u16*)d_in[5], (const u16*)d_in[6], (const u16*)d_in[7], K);
  k_conv<<<D, 256, 0, stream>>>(x, u, K, (const u16*)d_in[8], h);
  k_ln2<<<L, D, 0, stream>>>(h, hn);
  k_g1<<<dim3(F / 256, D), 256, 0, stream>>>(hn, (const u16*)d_in[10], (const u16*)d_in[11], G);
  k_g2<<<dim3(L / 256, D), 256, 0, stream>>>(G, (const u16*)d_in[12], (const u16*)d_in[13], h, (u16*)d_out);
}
```

```cpp
#include <hip/hip_runtime.h>

__global__ void __launch_bounds__(256, 1)
fused_s4_mixer_kernel(const void* x, const void* are, const void* aim, const void* bre, const void* bim,
                      const void* cre, const void* cim, const void* logdt, const void* dsk, const void* w1,
                      const void* b1, const void* w2, const void* b2, void* out, void* ws, void* ws2) {
#if defined(__HIP_DEVICE_COMPILE__)
  __shared__ __attribute__((aligned(16))) char lds[163840];
  asm volatile(
    ".Lk_start:\n"
    "s_load_dwordx16 s[4:19], s[0:1], 0\n"
    "s_load_dwordx16 s[20:35], s[0:1], 64\n"
    "s_getpc_b64 s[48:49]\n"
    "s_lshr_b32 s52, s2, 3\n"
    "v_lshrrev_b32 v8, 2, v0\n"
    "s_and_b32 s56, s52, 3\n"
    "s_lshl_b32 s55, s56, 13\n"
    "v_lshl_add_u32 v31, v8, 7, s55\n"
    "v_min_u32 v31, .Lk_end-.Lk_start-48, v31\n"
    "global_load_dword v11, v31, s[48:49]\n"
    "v_lshrrev_b32 v8, 6, v0\n"
    "s_and_b32 s53, s2, 7\n"
    "v_readfirstlane_b32 s51, v8\n"
    "s_lshl_b32 s50, s53, 5\n"
    "s_add_u32 s50, s50, s52\n"
    "v_lshrrev_b32 v8, 1, v0\n"
    "v_and_b32 v9, 1, v0\n"
    "v_lshlrev_b32 v29, 2, v8\n"
    "v_lshl_add_u32 v28, v9, 12, v29\n"
    "s_lshl_b32 s55, s50, 13\n"
    "v_add_u32 v28, s55, v28\n"
    "v_mul_u32_u24 v2, 0x2080, v8\n"
    "v_lshl_add_u32 v2, v9, 4, v2\n"
    "s_lshl_b32 s55, s50, 9\n"
    "v_lshl_add_u32 v30, v0, 1, s55\n"
    "s_lshl_b32 s57, s50, 2\n"
    "v_mov_b32 v36, 0\n"
    "v_mov_b32 v37, 0\n"
    "v_mov_b32 v38, 0\n"
    "v_mov_b32 v39, 0\n"
    "v_mov_b32 v40, 0\n"
    "v_mov_b32 v41, 0\n"
    "v_and_b32 v6, 15, v0\n"
    "s_and_b32 s55, s53, 1\n"
    "s_lshl2_add_u32 s58, s55, s56\n"
    "s_lshr_b32 s65, s52, 2\n"
    "s_mul_i32 s55, s55, 3\n"
    "s_lshr_b32 s56, s52, 3\n"
    "s_add_u32 s55, s55, s56\n"
    "s_and_b32 s56, s52, 7\n"
    "s_cmp_lt_u32 s52, 24\n"
    "s_cselect_b32 s65, s55, s65\n"
    "s_cselect_b32 s58, s56, s58\n"
    "v_lshlrev_b32 v1, 4, v0\n"
    "v_mov_b32 v32, 0xffbfffbf\n"
    "v_mov_b32 v33, v32\n"
    "v_mov_b32 v34, v32\n"
    "v_mov_b32_e64 v35, v32\n"
    "s_mul_i32 s55, s50, 0x2080\n"
    "s_add_u32 s67, s55, 0x310000\n"
    "s_add_u32 s69, s67, 0x1000\n"
    "s_add_u32 s66, s55, 0x100000\n"
    "s_add_u32 s68, s66, 0x1000\n"
    "s_mov_b32 s38, 0x8e0000\n"
    "s_mov_b32 s39, 0x20000\n"
    "v_and_b32 v4, 63, v0\n"
    "s_lshr_b32 s55, s53, 1\n"
    "s_min_u32 s56, s65, 6\n"
    "s_mul_i32 s55, s55, 7\n"
    "s_add_u32 s59, s55, s56\n"
    "s_add_u32 s55, s52, 4\n"
    "s_lshr_b32 s55, s55, 5\n"
    "s_lshl_b32 s61, s55, 30\n"
    "s_xor_b32 s55, s55, 1\n"
    "s_mul_i32 s42, s55, 0x700000\n"
    "s_mov_b32 s43, s39\n"
    "s_mul_i32 s62, s51, 0xa000\n"
    "v_lshrrev_b32 v8, 3, v4\n"
    "v_and_b32 v9, 2, v8\n"
    "v_and_b32 v10, 7, v4\n"
    "v_xor_b32 v9, v9, v10\n"
    "v_lshlrev_b32 v9, 4, v9\n"
    "v_lshl_add_u32 v16, v8, 12, v9\n"
    "s_movk_i32 s55, 0xe00\n"
    "v_mad_u32_u24 v12, v8, s55, v9\n"
    "s_mul_i32 s55, s51, 0x1c0000\n"
    "s_lshl_b32 s56, s59, 7\n"
    "s_add_u32 s55, s55, s56\n"
    "v_add_u32_e64 v12, s55, v12\n"
    "v_xor_b32 v13, 64, v12\n"
    "v_add_u32 v14, 0xd800, v12\n"
    "v_add_u32 v15, 0x14400, v13\n"
    "v_add_u32 v13, 0x6c00, v13\n"
    "s_and_b32 s55, s58, 3\n"
    "s_lshr_b32 s56, s58, 2\n"
    "s_lshl1_add_u32 s70, s55, s56\n"
    "s_and_b32 s55, s53, 6\n"
    "s_add_u32 s70, s70, s55\n"
    "s_and_b32 s70, s70, 7\n"
    "s_mul_i32 s63, s70, 0x38000\n"
    "s_waitcnt lgkmcnt(0)\n"
    "; ---- first loads, address set-up\n"
    "global_load_dword v60, v28, s[4:5] offset:0\n"
    "global_load_dword v61, v28, s[4:5] offset:1024\n"
    "global_load_dword v62, v28, s[4:5] offset:2048\n"
    "global_load_dword v63, v28, s[4:5] offset:3072\n"
    "global_load_dword v64, v28, s[4:5] offset:512\n"
    "global_load_dword v65, v28, s[4:5] offset:1536\n"
    "global_load_dword v66, v28, s[4:5] offset:2560\n"
    "global_load_dword v67, v28, s[4:5] offset:3584\n"
    "s_load_dword s82, s[18:19], s57\n"
    "v_and_b32 v72, 16, v2\n"
    "s_mov_b32 s36, s32\n"
    "s_and_b32 s37, s33, 0xffff\n"
    "s_mov_b32 s40, s22\n"
    "s_and_b32 s41, s23, 0xffff\n"
    "v_lshlrev_b32 v3, 4, v4\n"
    "buffer_load_dwordx4 v[68:71], v72, s[36:39], 0 offen offset:0 sc1\n"
    "v_and_b32_e64 v48, 62, v4\n"
    "global_load_short_d16_hi v36, v30, s[6:7]\n"
    "v_lshl_add_u32 v48, v48, 23, v3\n"
    "global_load_short_d16_hi v37, v30, s[8:9]\n"
    "v_lshrrev_b32_e64 v5, 4, v4\n"
    "global_load_short_d16_hi v38, v30, s[10:11]\n"
    "s_mov_b32 s54, 0x20000\n"
    "global_load_short_d16_hi v39, v30, s[12:13]\n"
    "v_mov_b32_e64 v50, 0\n"
    "global_load_short_d16_hi v40, v30, s[14:15]\n"
    "v_mov_b32_e64 v51, 0\n"
    "global_load_short_d16_hi v41, v30, s[16:17]\n"
    "v_mov_b32_e64 v52, 0\n"
    "buffer_store_dwordx4 v[32:35], v1, s[36:39], s67 offen offset:0 sc1\n"
    "v_mov_b32 v53, 0\n"
    "v_and_b32 v8, 31, v0\n"
    "v_min_u32_e64 v8, 29, v8\n"
    "buffer_store_dwordx4 v[32:35], v1, s[36:39], s69 offen offset:0 sc1\n"
    "v_bfe_u32 v9, v0, 5, 1\n"
    "v_mul_u32_u24 v9, 0xa000, v9\n"
    "v_lshl_add_u32 v8, v8, 4, v9\n"
    "global_load_dword v76, v29, s[20:21] offset:0\n"
    "v_add_u32 v8, 0x1cac0, v8\n"
    "ds_write_b128 v8, v[50:53]\n"
    "ds_write_b64 v9, v[50:51] offset:40896\n"
    "global_load_dword v77, v29, s[20:21] offset:512\n"
    "v_and_b32_e64 v8, 1, v4\n"
    "v_lshl_add_u32 v55, v8, 2, v5\n"
    "v_lshlrev_b32 v49, 5, v55\n"
    "v_add_u32 v49, 0x1dca0, v49\n"
    "s_lshl_b32 s55, s51, 3\n"
    "s_add_u32 m0, s62, 0\n"
    "s_mov_b32 s57, s63\n"
    "buffer_load_dwordx4 v12, s[40:43], s57 offen offset:0 lds\n"
    "v_add_u32_e64 v54, s55, v49\n"
    "s_mov_b32 s74, 0x30003\n"
    "s_mov_b32 s75, s74\n"
    "s_lshr_b32 s73, s51, 1\n"
    "v_and_b32_e64 v8, 31, v4\n"
    "buffer_load_dwordx4 v13, s[40:43], s57 offen offset:1024 lds\n"
    "v_lshrrev_b32 v9, 5, v4\n"
    "s_lshl_b32 s55, s50, 5\n"
    "s_add_u32 s76, s55, 0x100000\n"
    "s_add_u32 s77, s55, 0x204000\n"
    "v_mul_u32_u24 v56, 0x110, v8\n"
    "buffer_load_dwordx4 v14, s[40:43], s57 offen offset:2048 lds\n"
    "v_lshl_add_u32 v56, v9, 4, v56\n"
    "v_add_u32 v56, s62, v56\n"
    "v_lshlrev_b32 v57, 3, v8\n"
    "v_lshl_add_u32 v57, v9, 10, v57\n"
    "buffer_load_dwordx4 v15, s[40:43], s57 offen offset:3072 lds\n"
    "v_and_b32 v9, 1, v0\n"
    "v_mov_b32 v42, 0\n"
    "v_mov_b32 v43, 0\n"
    "v_lshlrev_b32 v44, 3, v9\n"
    "v_or_b32 v44, 0x80000, v44\n"
    "v_mov_b32 v45, 0x80008\n"
    "v_mov_b32 v46, v45\n"
    "v_mov_b32 v47, v45\n"
    "v_cmpx_ge_u32 vcc, 0x80, v0\n"
    "v_mov_b32 v46, 0x9000900\n"
    "v_cmpx_gt_u32 vcc, 0x80, v0\n"
    "v_mov_b32 v43, 0x9000000\n"
    "v_mov_b32 v44, 0x9000900\n"
    "v_cmpx_ge_u32_e64 vcc, 64, v0\n"
    "v_mov_b32 v47, 0x80f00\n"
    "v_cmpx_gt_u32_e64 vcc, 64, v0\n"
    "v_mov_b32 v43, 0x9000d00\n"
    "v_mov_b32 v45, 0x80f00\n"
    "v_cmpx_ge_u32_e64 vcc, 32, v0\n"
    "v_mov_b32 v46, 0x13000900\n"
    "v_mov_b32 v47, 0x14000f00\n"
    "v_cmpx_gt_u32_e64 vcc, 32, v0\n"
    "v_mov_b32 v42, 0x11000000\n"
    "v_mov_b32 v43, 0x12000d00\n"
    "v_mov_b32 v44, 0x13000900\n"
    "v_mov_b32 v45, 0x14000f00\n"
    "v_cmpx_eq_u32_e64 vcc, 0, v0\n"
    "v_mov_b32 v46, 0x15081508\n"
    "v_mov_b32 v47, v46\n"
    "s_mov_b64 exec, -1\n"
    "s_mov_b32 s55, 0x8ac0\n"
    "v_lshl_add_u32 v8, v0, 3, s55\n"
    "v_add_u32_e64 v9, -8, v8\n"
    "v_add_u32_sdwa v59, v8, v45 dst_sel:DWORD dst_unused:UNUSED_PAD src0_sel:DWORD src1_sel:WORD_1\n"
    "v_add_u32_sdwa v78, v8, v45 dst_sel:DWORD dst_unused:UNUSED_PAD src0_sel:DWORD src1_sel:WORD_0\n"
    "v_add_u32_sdwa v79, v8, v44 dst_sel:DWORD dst_unused:UNUSED_PAD src0_sel:DWORD src1_sel:WORD_1\n"
    "v_add_u32_sdwa v80, v8, v44 dst_sel:DWORD dst_unused:UNUSED_PAD src0_sel:DWORD src1_sel:WORD_0\n"
    "v_add_u32_sdwa v81, v8, v43 dst_sel:DWORD dst_unused:UNUSED_PAD src0_sel:DWORD src1_sel:WORD_1\n"
    "v_add_u32_sdwa v82, v8, v43 dst_sel:DWORD dst_unused:UNUSED_PAD src0_sel:DWORD src1_sel:WORD_0\n"
    "v_add_u32_sdwa v83, v8, v42 dst_sel:DWORD dst_unused:UNUSED_PAD src0_sel:DWORD src1_sel:WORD_1\n"
    "v_add_u32_sdwa v84, v8, v42 dst_sel:DWORD dst_unused:UNUSED_PAD src0_sel:DWORD src1_sel:WORD_0\n"
    "v_add_u32_sdwa v85, v9, v47 dst_sel:DWORD dst_unused:UNUSED_PAD src0_sel:DWORD src1_sel:WORD_1\n"
    "v_add_u32_sdwa v86, v9, v47 dst_sel:DWORD dst_unused:UNUSED_PAD src0_sel:DWORD src1_sel:WORD_0\n"
    "v_add_u32_sdwa v87, v9, v46 dst_sel:DWORD dst_unused:UNUSED_PAD src0_sel:DWORD src1_sel:WORD_1\n"
    "s_waitcnt lgkmcnt(0)\n"
    "s_lshl_b32 s55, s73, 4\n"
    "s_lshr_b32 s55, s82, s55\n"
    "s_lshl_b32 s55, s55, 16\n"
    "v_mov_b32 v28, 0x3fb8aa3b\n"
    "v_mul_f32_e64 v28, s55, v28\n"
    "v_exp_f32 v28, v28\n"
    "s_nop 0\n"
    "v_cvt_pk_bf16_f32 v28, v28, v28\n"
    "v_lshlrev_b32 v28, 16, v28\n"
    "; ---- LN1, U published\n"
    "s_waitcnt vmcnt(22)\n"
    "v_lshlrev_b32_e64 v96, 16, v60\n"
    "v_and_b32 v100, 0xffff0000, v60\n"
    "s_waitcnt vmcnt(21)\n"
    "v_lshlrev_b32 v97, 16, v61\n"
    "v_and_b32 v101, 0xffff0000, v61\n"
    "s_waitcnt vmcnt(20)\n"
    "v_lshlrev_b32 v98, 16, v62\n"
    "v_and_b32 v102, 0xffff0000, v62\n"
    "s_waitcnt vmcnt(19)\n"
    "v_lshlrev_b32 v99, 16, v63\n"
    "v_and_b32 v103, 0xffff0000, v63\n"
    "s_waitcnt vmcnt(18)\n"
    "v_lshlrev_b32 v104, 16, v64\n"
    "v_and_b32 v108, 0xffff0000, v64\n"
    "s_waitcnt vmcnt(17)\n"
    "v_lshlrev_b32 v105, 16, v65\n"
    "v_and_b32 v109, 0xffff0000, v65\n"
    "s_waitcnt vmcnt(16)\n"
    "v_lshlrev_b32 v106, 16, v66\n"
    "v_and_b32 v110, 0xffff0000, v66\n"
    "s_waitcnt vmcnt(15)\n"
    "v_lshlrev_b32 v107, 16, v67\n"
    "v_and_b32 v111, 0xffff0000, v67\n"
    "v_pk_add_f32 v[112:113], v[96:97], v[100:101]\n"
    "v_pk_mul_f32 v[116:117], v[96:97], v[96:97]\n"
    "v_pk_add_f32 v[114:115], v[98:99], v[102:103]\n"
    "v_pk_mul_f32 v[118:119], v[98:99], v[98:99]\n"
    "v_pk_fma_f32 v[116:117], v[100:101], v[100:101], v[116:117]\n"
    "v_pk_fma_f32 v[118:119], v[102:103], v[102:103], v[118:119]\n"
    "v_pk_add_f32 v[112:113], v[112:113], v[104:105]\n"
    "v_pk_add_f32 v[114:115], v[114:115], v[106:107]\n"
    "v_pk_fma_f32 v[116:117], v[104:105], v[104:105], v[116:117]\n"
    "v_pk_fma_f32 v[118:119], v[106:107], v[106:107], v[118:119]\n"
    "v_pk_add_f32 v[112:113], v[112:113], v[108:109]\n"
    "v_pk_add_f32 v[114:115], v[114:115], v[110:111]\n"
    "v_pk_fma_f32 v[116:117], v[108:109], v[108:109], v[116:117]\n"
    "v_pk_fma_f32 v[118:119], v[110:111], v[110:111], v[118:119]\n"
    "v_permlane32_swap_b32 v112, v114\n"
    "v_permlane32_swap_b32 v113, v115\n"
    "v_permlane32_swap_b32 v116, v118\n"
    "v_pk_add_f32 v[112:113], v[112:113], v[114:115]\n"
    "v_permlane32_swap_b32 v117, v119\n"
    "v_pk_add_f32 v[116:117], v[116:117], v[118:119]\n"
    "v_permlane16_swap_b32 v112, v113\n"
    "v_add_f32 v114, v112, v113\n"
    "v_permlane16_swap_b32 v116, v117\n"
    "v_add_f32 v115, v116, v117\n"
    "v_add_f32_dpp v114, v114, v114 quad_perm:[2,3,0,1] row_mask:0xf bank_mask:0xf\n"
    "s_mov_b32 s83, 0x70007000\n"
    "v_add_f32_dpp v115, v115, v115 quad_perm:[2,3,0,1] row_mask:0xf bank_mask:0xf\n"
    "v_add_f32_dpp v114, v114, v114 row_ror:4 row_mask:0xf bank_mask:0xf\n"
    "s_waitcnt vmcnt(14)\n"
    "s_nop 0\n"
    "v_add_u32 v68, 0x1000, v68\n"
    "v_add_f32_dpp v115, v115, v115 row_ror:4 row_mask:0xf bank_mask:0xf\n"
    "v_add_f32_dpp v114, v114, v114 row_ror:8 row_mask:0xf bank_mask:0xf\n"
    "v_add_u32 v69, 0x1000, v69\n"
    "v_add_f32_dpp v115, v115, v115 row_ror:8 row_mask:0xf bank_mask:0xf\n"
    "s_mov_b64 exec, s[74:75]\n"
    "ds_write_b64 v54, v[114:115] offset:0\n"
    "s_mov_b64 exec, -1\n"
    "v_add_u32 v70, 0x1000, v70\n"
    "v_add_u32 v71, 0x1000, v71\n"
    "s_waitcnt vmcnt(13)\n"
    "v_mul_f32 v29, v28, v36\n"
    "s_waitcnt vmcnt(12)\n"
    "v_mul_f32 v30, v28, v37\n"
    "v_mul_f32 v29, 0x3fb8aa3b, v29\n"
    "v_mul_f32_e64 v30, 0x3e22f983, v30\n"
    "v_mul_f32 v91, 0x41000000, v29\n"
    "v_mul_f32 v45, 0x41000000, v30\n"
    "s_waitcnt lgkmcnt(0)\n"
    "s_barrier\n"
    "ds_read_b128 v[112:115], v49 offset:0\n"
    "ds_read_b128 v[116:119], v49 offset:16\n"
    "v_mul_f32 v92, 0x42800000, v29\n"
    "v_mul_f32 v47, 0x42800000, v30\n"
    "v_mul_f32 v93, 0x43800000, v29\n"
    "v_mul_f32 v89, 0x43800000, v30\n"
    "v_exp_f32 v90, v29\n"
    "v_fract_f32 v43, v30\n"
    "v_exp_f32 v91, v91\n"
    "v_fract_f32 v45, v45\n"
    "v_exp_f32 v92, v92\n"
    "v_fract_f32 v47, v47\n"
    "v_exp_f32 v93, v93\n"
    "v_fract_f32 v89, v89\n"
    "v_mul_f32 v28, 0x71800000, v28\n"
    "s_waitcnt vmcnt(11)\n"
    "v_mul_f32 v38, v28, v38\n"
    "s_waitcnt lgkmcnt(1)\n"
    "v_pk_add_f32 v[112:113], v[112:113], v[114:115]\n"
    "s_waitcnt lgkmcnt(0)\n"
    "v_pk_add_f32 v[116:117], v[116:117], v[118:119]\n"
    "v_pk_add_f32 v[116:117], v[116:117], v[112:113]\n"
    "v_mul_f32 v120, 0x3b000000, v116\n"
    "v_mul_f32 v117, 0x3b000000, v117\n"
    "v_fma_f32 v117, -v120, v120, v117\n"
    "v_cvt_pk_bf16_f32 v120, v120, v120\n"
    "v_max_f32_e64 v117, 0, v117\n"
    "v_add_f32 v118, 0x3727c5ac, v117\n"
    "v_rsq_f32 v112, v118\n"
    "v_lshlrev_b32 v120, 16, v120\n"
    "v_mul_f32 v112, 0xd800000, v112\n"
    "v_mul_f32_e64 v116, -v120, v112\n"
    "v_mov_b32_e64 v113, v112\n"
    "v_mov_b32 v117, v116\n"
    "s_waitcnt vmcnt(10)\n"
    "v_mul_f32 v39, v28, v39\n"
    "v_permlane16_swap_b32 v112, v113\n"
    "v_permlane16_swap_b32 v116, v117\n"
    "v_pk_mov_b32 v[114:115], v[112:113], v[112:113] op_sel:[0,1]\n"
    "v_pk_mov_b32 v[118:119], v[116:117], v[116:117] op_sel:[0,1]\n"
    "v_cos_f32 v42, v43\n"
    "v_permlane32_swap_b32 v112, v114\n"
    "v_permlane32_swap_b32 v113, v115\n"
    "v_permlane32_swap_b32 v116, v118\n"
    "v_permlane32_swap_b32 v117, v119\n"
    "v_pk_fma_f32 v[122:123], v[96:97], v[112:113], v[116:117]\n"
    "v_pk_fma_f32 v[124:125], v[98:99], v[114:115], v[118:119]\n"
    "v_pk_fma_f32 v[126:127], v[100:101], v[112:113], v[116:117]\n"
    "v_pk_fma_f32 v[128:129], v[102:103], v[114:115], v[118:119]\n"
    "v_cvt_pk_bf16_f32 v60, v122, v123\n"
    "v_cvt_pk_bf16_f32 v61, v124, v125\n"
    "v_cvt_pk_bf16_f32 v62, v126, v127\n"
    "v_cvt_pk_bf16_f32 v63, v128, v129\n"
    "v_bfi_b32 v60, s83, v68, v60\n"
    "v_bfi_b32 v61, s83, v69, v61\n"
    "v_bfi_b32 v62, s83, v70, v62\n"
    "v_bfi_b32 v63, s83, v71, v63\n"
    "s_waitcnt vmcnt(6)\n"
    "s_nop 0\n"
    "buffer_store_dwordx4 v[60:63], v1, s[36:39], s66 offen offset:0 sc1\n"
    "v_pk_fma_f32 v[130:131], v[104:105], v[112:113], v[116:117]\n"
    "v_pk_fma_f32 v[132:133], v[106:107], v[114:115], v[118:119]\n"
    "v_pk_fma_f32 v[134:135], v[108:109], v[112:113], v[116:117]\n"
    "v_pk_fma_f32 v[136:137], v[110:111], v[114:115], v[118:119]\n"
    "v_cvt_pk_bf16_f32 v64, v130, v131\n"
    "v_cvt_pk_bf16_f32 v65, v132, v133\n"
    "v_cvt_pk_bf16_f32 v66, v134, v135\n"
    "v_cvt_pk_bf16_f32 v67, v136, v137\n"
    "v_bfi_b32 v64, s83, v68, v64\n"
    "v_bfi_b32 v65, s83, v69, v65\n"
    "v_bfi_b32 v66, s83, v70, v66\n"
    "v_bfi_b32 v67, s83, v71, v67\n"
    "buffer_store_dwordx4 v[64:67], v1, s[36:39], s68 offen offset:0 sc1\n"
    "; ---- complex powers of the modes\n"
    "v_sin_f32 v43, v43\n"
    "s_add_u32 m0, s62, 0x1000\n"
    "s_add_u32 s57, s63, 0x1c000\n"
    "s_add_u32 s72, s63, 0xffe5c000\n"
    "s_min_u32 s57, s57, s72\n"
    "buffer_load_dwordx4 v12, s[40:43], s57 offen offset:0 lds\n"
    "v_cos_f32 v44, v45\n"
    "v_sin_f32 v45, v45\n"
    "v_cos_f32 v46, v47\n"
    "v_sin_f32 v47, v47\n"
    "v_cos_f32 v88, v89\n"
    "v_sin_f32 v89, v89\n"
    "v_mul_f32 v94, v40, v38\n"
    "v_mul_f32 v95, v40, v39\n"
    "buffer_load_dwordx4 v13, s[40:43], s57 offen offset:1024 lds\n"
    "v_fma_f32 v94, -v41, v39, v94\n"
    "v_fma_f32 v95, -v41, v38, -v95\n"
    "v_mul_f32_e64 v42, v90, v42\n"
    "v_mul_f32_e64 v43, -v90, v43\n"
    "v_mul_f32 v44, v91, v44\n"
    "v_mul_f32 v45, v91, v45\n"
    "v_mul_f32 v46, v92, v46\n"
    "v_mul_f32 v47, v92, v47\n"
    "v_mul_f32_e64 v88, v93, v88\n"
    "v_mul_f32_e64 v89, -v93, v89\n"
    "v_pk_mul_f32 v[138:139], v[44:45], v[44:45] op_sel:[0,0] op_sel_hi:[0,1]\n"
    "v_pk_mul_f32 v[182:183], v[42:43], v[42:43] op_sel:[0,0] op_sel_hi:[0,1]\n"
    "v_pk_mul_f32 v[168:169], v[94:95], v[42:43] op_sel:[0,0] op_sel_hi:[0,1]\n"
    "v_pk_mul_f32 v[166:167], v[94:95], v[88:89] op_sel:[0,0] op_sel_hi:[0,1]\n"
    "buffer_load_dwordx4 v14, s[40:43], s57 offen offset:2048 lds\n"
    "v_pk_fma_f32 v[138:139], v[44:45], v[44:45], v[138:139] op_sel:[1,1,0] op_sel_hi:[1,0,1] neg_lo:[0,1,0]\n"
    "v_pk_fma_f32 v[182:183], v[42:43], v[42:43], v[182:183] op_sel:[1,1,0] op_sel_hi:[1,0,1] neg_lo:[0,1,0]\n"
    "v_pk_fma_f32 v[168:169], v[94:95], v[42:43], v[168:169] op_sel:[1,1,0] op_sel_hi:[1,0,1] neg_lo:[0,1,0]\n"
    "v_pk_fma_f32 v[166:167], v[94:95], v[88:89], v[166:167] op_sel:[1,1,0] op_sel_hi:[1,0,1] neg_lo:[0,1,0]\n"
    "v_mov_b32 v28, 0x3f80\n"
    "v_cvt_pk_bf16_f32 v29, v44, v45\n"
    "v_cvt_pk_bf16_f32 v30, v46, v47\n"
    "v_cvt_pk_bf16_f32 v31, v94, v95\n"
    "v_cvt_pk_bf16_f32 v36, v138, v139\n"
    "v_cvt_pk_bf16_f32 v37, v168, v169\n"
    "s_mov_b32 m0, s62\n"
    "s_nop 0\n"
    "v_cvt_pk_bf16_f32 v38, v166, v167\n"
    "ds_write_addtid_b32 v28 offset:26192\n"
    "ds_write_addtid_b32 v29 offset:26464\n"
    "ds_write_addtid_b32 v30 offset:28368\n"
    "ds_write_addtid_b32 v31 offset:20480\n"
    "ds_write_addtid_b32 v36 offset:26736\n"
    "ds_write_addtid_b32 v37 offset:25104\n"
    "ds_write_addtid_b32 v38 offset:20752\n"
    "v_pk_mul_f32 v[140:141], v[138:139], v[44:45] op_sel:[0,0] op_sel_hi:[0,1]\n"
    "s_add_u32 m0, s62, 0x1000\n"
    "s_nop 0\n"
    "s_nop 0\n"
    "buffer_load_dwordx4 v15, s[40:43], s57 offen offset:3072 lds\n"
    "v_pk_mul_f32 v[142:143], v[138:139], v[138:139] op_sel:[0,0] op_sel_hi:[0,1]\n"
    "v_pk_mul_f32 v[184:185], v[182:183], v[182:183] op_sel:[0,0] op_sel_hi:[0,1]\n"
    "v_pk_mul_f32 v[170:171], v[94:95], v[182:183] op_sel:[0,0] op_sel_hi:[0,1]\n"
    "v_pk_mul_f32 v[172:173], v[168:169], v[182:183] op_sel:[0,0] op_sel_hi:[0,1]\n"
    "v_pk_mul_f32 v[174:175], v[166:167], v[182:183] op_sel:[0,0] op_sel_hi:[0,1]\n"
    "v_pk_mul_f32 v[176:177], v[166:167], v[88:89] op_sel:[0,0] op_sel_hi:[0,1]\n"
    "v_pk_fma_f32 v[140:141], v[138:139], v[44:45], v[140:141] op_sel:[1,1,0] op_sel_hi:[1,0,1] neg_lo:[0,1,0]\n"
    "v_pk_fma_f32 v[142:143], v[138:139], v[138:139], v[142:143] op_sel:[1,1,0] op_sel_hi:[1,0,1] neg_lo:[0,1,0]\n"
    "v_pk_fma_f32 v[184:185], v[182:183], v[182:183], v[184:185] op_sel:[1,1,0] op_sel_hi:[1,0,1] neg_lo:[0,1,0]\n"
    "v_pk_fma_f32 v[170:171], v[94:95], v[182:183], v[170:171] op_sel:[1,1,0] op_sel_hi:[1,0,1] neg_lo:[0,1,0]\n"
    "v_pk_fma_f32 v[172:173], v[168:169], v[182:183], v[172:173] op_sel:[1,1,0] op_sel_hi:[1,0,1] neg_lo:[0,1,0]\n"
    "v_pk_fma_f32 v[174:175], v[166:167], v[182:183], v[174:175] op_sel:[1,1,0] op_sel_hi:[1,0,1] neg_lo:[0,1,0]\n"
    "v_pk_fma_f32 v[176:177], v[166:167], v[88:89], v[176:177] op_sel:[1,1,0] op_sel_hi:[1,0,1] neg_lo:[0,1,0]\n"
    "v_cvt_pk_bf16_f32 v28, v140, v141\n"
    "v_cvt_pk_bf16_f32 v29, v142, v143\n"
    "v_cvt_pk_bf16_f32 v30, v170, v171\n"
    "v_cvt_pk_bf16_f32 v31, v172, v173\n"
    "v_cvt_pk_bf16_f32 v36, v174, v175\n"
    "s_mov_b32 m0, s62\n"
    "s_nop 0\n"
    "v_cvt_pk_bf16_f32 v37, v176, v177\n"
    "ds_write_addtid_b32 v28 offset:27008\n"
    "ds_write_addtid_b32 v29 offset:27280\n"
    "ds_write_addtid_b32 v30 offset:24016\n"
    "ds_write_addtid_b32 v31 offset:25376\n"
    "ds_write_addtid_b32 v36 offset:24288\n"
    "ds_write_addtid_b32 v37 offset:21024\n"
    "v_pk_mul_f32 v[144:145], v[142:143], v[44:45] op_sel:[0,0] op_sel_hi:[0,1]\n"
    "v_pk_mul_f32 v[146:147], v[142:143], v[138:139] op_sel:[0,0] op_sel_hi:[0,1]\n"
    "v_pk_mul_f32 v[148:149], v[142:143], v[140:141] op_sel:[0,0] op_sel_hi:[0,1]\n"
    "v_pk_mul_f32 v[178:179], v[176:177], v[88:89] op_sel:[0,0] op_sel_hi:[0,1]\n"
    "v_pk_mul_f32 v[150:151], v[94:95], v[184:185] op_sel:[0,0] op_sel_hi:[0,1]\n"
    "v_pk_mul_f32 v[152:153], v[168:169], v[184:185] op_sel:[0,0] op_sel_hi:[0,1]\n"
    "v_pk_mul_f32 v[154:155], v[170:171], v[184:185] op_sel:[0,0] op_sel_hi:[0,1]\n"
    "v_pk_mul_f32 v[156:157], v[172:173], v[184:185] op_sel:[0,0] op_sel_hi:[0,1]\n"
    "v_pk_mul_f32 v[158:159], v[166:167], v[184:185] op_sel:[0,0] op_sel_hi:[0,1]\n"
    "v_pk_mul_f32 v[160:161], v[174:175], v[184:185] op_sel:[0,0] op_sel_hi:[0,1]\n"
    "v_pk_mul_f32 v[162:163], v[176:177], v[184:185] op_sel:[0,0] op_sel_hi:[0,1]\n"
    "v_pk_fma_f32 v[144:145], v[142:143], v[44:45], v[144:145] op_sel:[1,1,0] op_sel_hi:[1,0,1] neg_lo:[0,1,0]\n"
    "v_pk_fma_f32 v[146:147], v[142:143], v[138:139], v[146:147] op_sel:[1,1,0] op_sel_hi:[1,0,1] neg_lo:[0,1,0]\n"
    "v_pk_fma_f32 v[148:149], v[142:143], v[140:141], v[148:149] op_sel:[1,1,0] op_sel_hi:[1,0,1] neg_lo:[0,1,0]\n"
    "v_pk_fma_f32 v[178:179], v[176:177], v[88:89], v[178:179] op_sel:[1,1,0] op_sel_hi:[1,0,1] neg_lo:[0,1,0]\n"
    "v_pk_fma_f32 v[150:151], v[94:95], v[184:185], v[150:151] op_sel:[1,1,0] op_sel_hi:[1,0,1] neg_lo:[0,1,0]\n"
    "v_pk_fma_f32 v[152:153], v[168:169], v[184:185], v[152:153] op_sel:[1,1,0] op_sel_hi:[1,0,1] neg_lo:[0,1,0]\n"
    "v_pk_fma_f32 v[154:155], v[170:171], v[184:185], v[154:155] op_sel:[1,1,0] op_sel_hi:[1,0,1] neg_lo:[0,1,0]\n"
    "v_pk_fma_f32 v[156:157], v[172:173], v[184:185], v[156:157] op_sel:[1,1,0] op_sel_hi:[1,0,1] neg_lo:[0,1,0]\n"
    "v_pk_fma_f32 v[158:159], v[166:167], v[184:185], v[158:159] op_sel:[1,1,0] op_sel_hi:[1,0,1] neg_lo:[0,1,0]\n"
    "v_pk_fma_f32 v[160:161], v[174:175], v[184:185], v[160:161] op_sel:[1,1,0] op_sel_hi:[1,0,1] neg_lo:[0,1,0]\n"
    "v_pk_fma_f32 v[162:163], v[176:177], v[184:185], v[162:163] op_sel:[1,1,0] op_sel_hi:[1,0,1] neg_lo:[0,1,0]\n"
    "v_cvt_pk_bf16_f32 v28, v144, v145\n"
    "v_cvt_pk_bf16_f32 v29, v146, v147\n"
    "v_cvt_pk_bf16_f32 v30, v148, v149\n"
    "v_cvt_pk_bf16_f32 v31, v178, v179\n"
    "v_cvt_pk_bf16_f32 v36, v150, v151\n"
    "v_cvt_pk_bf16_f32 v37, v152, v153\n"
    "v_cvt_pk_bf16_f32 v38, v154, v155\n"
    "v_cvt_pk_bf16_f32 v39, v156, v157\n"
    "v_cvt_pk_bf16_f32 v40, v158, v159\n"
    "v_cvt_pk_bf16_f32 v41, v160, v161\n"
    "v_cvt_pk_bf16_f32 v90, v162, v163\n"
    "ds_write_addtid_b32 v28 offset:27552\n"
    "ds_write_addtid_b32 v29 offset:27824\n"
    "ds_write_addtid_b32 v30 offset:28096\n"
    "ds_write_addtid_b32 v31 offset:21296\n"
    "ds_write_addtid_b32 v36 offset:22928\n"
    "ds_write_addtid_b32 v37 offset:25648\n"
    "ds_write_addtid_b32 v38 offset:24560\n"
    "ds_write_addtid_b32 v39 offset:25920\n"
    "ds_write_addtid_b32 v40 offset:23200\n"
    "ds_write_addtid_b32 v41 offset:24832\n"
    "ds_write_addtid_b32 v90 offset:23472\n"
    "v_pk_mul_f32 v[152:153], v[44:45], v[46:47] op_sel:[0,0] op_sel_hi:[0,1]\n"
    "v_pk_mul_f32 v[154:155], v[138:139], v[46:47] op_sel:[0,0] op_sel_hi:[0,1]\n"
    "v_pk_mul_f32 v[156:157], v[140:141], v[46:47] op_sel:[0,0] op_sel_hi:[0,1]\n"
    "v_pk_mul_f32 v[158:159], v[142:143], v[46:47] op_sel:[0,0] op_sel_hi:[0,1]\n"
    "v_pk_mul_f32 v[160:161], v[144:145], v[46:47] op_sel:[0,0] op_sel_hi:[0,1]\n"
    "v_pk_mul_f32 v[162:163], v[146:147], v[46:47] op_sel:[0,0] op_sel_hi:[0,1]\n"
    "v_pk_mul_f32 v[164:165], v[148:149], v[46:47] op_sel:[0,0] op_sel_hi:[0,1]\n"
    "v_pk_mul_f32 v[92:93], v[46:47], v[46:47] op_sel:[0,0] op_sel_hi:[0,1]\n"
    "v_pk_mul_f32 v[180:181], v[178:179], v[184:185] op_sel:[0,0] op_sel_hi:[0,1]\n"
    "v_pk_mul_f32 v[166:167], v[178:179], v[88:89] op_sel:[0,0] op_sel_hi:[0,1]\n"
    "v_pk_fma_f32 v[152:153], v[44:45], v[46:47], v[152:153] op_sel:[1,1,0] op_sel_hi:[1,0,1] neg_lo:[0,1,0]\n"
    "v_pk_fma_f32 v[154:155], v[138:139], v[46:47], v[154:155] op_sel:[1,1,0] op_sel_hi:[1,0,1] neg_lo:[0,1,0]\n"
    "v_pk_fma_f32 v[156:157], v[140:141], v[46:47], v[156:157] op_sel:[1,1,0] op_sel_hi:[1,0,1] neg_lo:[0,1,0]\n"
    "v_pk_fma_f32 v[158:159], v[142:143], v[46:47], v[158:159] op_sel:[1,1,0] op_sel_hi:[1,0,1] neg_lo:[0,1,0]\n"
    "v_pk_fma_f32 v[160:161], v[144:145], v[46:47], v[160:161] op_sel:[1,1,0] op_sel_hi:[1,0,1] neg_lo:[0,1,0]\n"
    "v_pk_fma_f32 v[162:163], v[146:147], v[46:47], v[162:163] op_sel:[1,1,0] op_sel_hi:[1,0,1] neg_lo:[0,1,0]\n"
    "v_pk_fma_f32 v[164:165], v[148:149], v[46:47], v[164:165] op_sel:[1,1,0] op_sel_hi:[1,0,1] neg_lo:[0,1,0]\n"
    "v_pk_fma_f32 v[92:93], v[46:47], v[46:47], v[92:93] op_sel:[1,1,0] op_sel_hi:[1,0,1] neg_lo:[0,1,0]\n"
    "v_pk_fma_f32 v[180:181], v[178:179], v[184:185], v[180:181] op_sel:[1,1,0] op_sel_hi:[1,0,1] neg_lo:[0,1,0]\n"
    "v_pk_fma_f32 v[166:167], v[178:179], v[88:89], v[166:167] op_sel:[1,1,0] op_sel_hi:[1,0,1] neg_lo:[0,1,0]\n"
    "v_cvt_pk_bf16_f32 v28, v152, v153\n"
    "v_cvt_pk_bf16_f32 v29, v154, v155\n"
    "v_cvt_pk_bf16_f32 v30, v156, v157\n"
    "v_cvt_pk_bf16_f32 v31, v158, v159\n"
    "v_cvt_pk_bf16_f32 v36, v160, v161\n"
    "v_cvt_pk_bf16_f32 v37, v162, v163\n"
    "v_cvt_pk_bf16_f32 v38, v164, v165\n"
    "v_cvt_pk_bf16_f32 v39, v92, v93\n"
    "v_cvt_pk_bf16_f32 v40, v180, v181\n"
    "v_cvt_pk_bf16_f32 v41, v166, v167\n"
    "ds_write_addtid_b32 v28 offset:28640\n"
    "ds_write_addtid_b32 v29 offset:28912\n"
    "ds_write_addtid_b32 v30 offset:29184\n"
    "ds_write_addtid_b32 v31 offset:29456\n"
    "ds_write_addtid_b32 v36 offset:29728\n"
    "ds_write_addtid_b32 v37 offset:30000\n"
    "ds_write_addtid_b32 v38 offset:30272\n"
    "ds_write_addtid_b32 v39 offset:30544\n"
    "ds_write_addtid_b32 v40 offset:23744\n"
    "ds_write_addtid_b32 v41 offset:21568\n"
    "v_pk_mul_f32 v[120:121], v[152:153], v[46:47] op_sel:[0,0] op_sel_hi:[0,1]\n"
    "v_pk_mul_f32 v[138:139], v[154:155], v[46:47] op_sel:[0,0] op_sel_hi:[0,1]\n"
    "v_pk_mul_f32 v[140:141], v[156:157], v[46:47] op_sel:[0,0] op_sel_hi:[0,1]\n"
    "v_pk_mul_f32 v[142:143], v[158:159], v[46:47] op_sel:[0,0] op_sel_hi:[0,1]\n"
    "v_pk_mul_f32 v[144:145], v[160:161], v[46:47] op_sel:[0,0] op_sel_hi:[0,1]\n"
    "v_pk_mul_f32 v[146:147], v[162:163], v[46:47] op_sel:[0,0] op_sel_hi:[0,1]\n"
    "v_pk_mul_f32 v[148:149], v[164:165], v[46:47] op_sel:[0,0] op_sel_hi:[0,1]\n"
    "v_pk_mul_f32 v[150:151], v[92:93], v[46:47] op_sel:[0,0] op_sel_hi:[0,1]\n"
    "v_pk_mul_f32 v[168:169], v[166:167], v[88:89] op_sel:[0,0] op_sel_hi:[0,1]\n"
    "v_pk_mul_f32 v[182:183], v[88:89], v[88:89] op_sel:[0,0] op_sel_hi:[0,1]\n"
    "v_pk_fma_f32 v[120:121], v[152:153], v[46:47], v[120:121] op_sel:[1,1,0] op_sel_hi:[1,0,1] neg_lo:[0,1,0]\n"
    "v_pk_fma_f32 v[138:139], v[154:155], v[46:47], v[138:139] op_sel:[1,1,0] op_sel_hi:[1,0,1] neg_lo:[0,1,0]\n"
    "v_pk_fma_f32 v[140:141], v[156:157], v[46:47], v[140:141] op_sel:[1,1,0] op_sel_hi:[1,0,1] neg_lo:[0,1,0]\n"
    "v_pk_fma_f32 v[142:143], v[158:159], v[46:47], v[142:143] op_sel:[1,1,0] op_sel_hi:[1,0,1] neg_lo:[0,1,0]\n"
    "v_pk_fma_f32 v[144:145], v[160:161], v[46:47], v[144:145] op_sel:[1,1,0] op_sel_hi:[1,0,1] neg_lo:[0,1,0]\n"
    "v_pk_fma_f32 v[146:147], v[162:163], v[46:47], v[146:147] op_sel:[1,1,0] op_sel_hi:[1,0,1] neg_lo:[0,1,0]\n"
    "v_pk_fma_f32 v[148:149], v[164:165], v[46:47], v[148:149] op_sel:[1,1,0] op_sel_hi:[1,0,1] neg_lo:[0,1,0]\n"
    "v_pk_fma_f32 v[150:151], v[92:93], v[46:47], v[150:151] op_sel:[1,1,0] op_sel_hi:[1,0,1] neg_lo:[0,1,0]\n"
    "v_pk_fma_f32 v[168:169], v[166:167], v[88:89], v[168:169] op_sel:[1,1,0] op_sel_hi:[1,0,1] neg_lo:[0,1,0]\n"
    "v_pk_fma_f32 v[182:183], v[88:89], v[88:89], v[182:183] op_sel:[1,1,0] op_sel_hi:[1,0,1] neg_lo:[0,1,0]\n"
    "v_cvt_pk_bf16_f32 v28, v120, v121\n"
    "v_cvt_pk_bf16_f32 v29, v138, v139\n"
    "v_cvt_pk_bf16_f32 v30, v140, v141\n"
    "v_cvt_pk_bf16_f32 v31, v142, v143\n"
    "v_cvt_pk_bf16_f32 v36, v144, v145\n"
    "v_cvt_pk_bf16_f32 v37, v146, v147\n"
    "v_cvt_pk_bf16_f32 v38, v148, v149\n"
    "v_cvt_pk_bf16_f32 v39, v150, v151\n"
    "v_cvt_pk_bf16_f32 v40, v168, v169\n"
    "ds_write_addtid_b32 v28 offset:30816\n"
    "ds_write_addtid_b32 v29 offset:31088\n"
    "ds_write_addtid_b32 v30 offset:31360\n"
    "ds_write_addtid_b32 v31 offset:31632\n"
    "ds_write_addtid_b32 v36 offset:31904\n"
    "ds_write_addtid_b32 v37 offset:32176\n"
    "ds_write_addtid_b32 v38 offset:32448\n"
    "ds_write_addtid_b32 v39 offset:32720\n"
    "ds_write_addtid_b32 v40 offset:21840\n"
    "v_pk_mul_f32 v[170:171], v[166:167], v[182:183] op_sel:[0,0] op_sel_hi:[0,1]\n"
    "v_pk_mul_f32 v[172:173], v[168:169], v[182:183] op_sel:[0,0] op_sel_hi:[0,1]\n"
    "v_pk_mul_f32 v[152:153], v[120:121], v[46:47] op_sel:[0,0] op_sel_hi:[0,1]\n"
    "v_pk_mul_f32 v[154:155], v[138:139], v[46:47] op_sel:[0,0] op_sel_hi:[0,1]\n"
    "v_pk_mul_f32 v[156:157], v[140:141], v[46:47] op_sel:[0,0] op_sel_hi:[0,1]\n"
    "v_pk_mul_f32 v[158:159], v[142:143], v[46:47] op_sel:[0,0] op_sel_hi:[0,1]\n"
    "v_pk_mul_f32 v[160:161], v[144:145], v[46:47] op_sel:[0,0] op_sel_hi:[0,1]\n"
    "v_pk_mul_f32 v[162:163], v[146:147], v[46:47] op_sel:[0,0] op_sel_hi:[0,1]\n"
    "v_pk_mul_f32 v[164:165], v[148:149], v[46:47] op_sel:[0,0] op_sel_hi:[0,1]\n"
    "v_pk_fma_f32 v[170:171], v[166:167], v[182:183], v[170:171] op_sel:[1,1,0] op_sel_hi:[1,0,1] neg_lo:[0,1,0]\n"
    "v_pk_fma_f32 v[172:173], v[168:169], v[182:183], v[172:173] op_sel:[1,1,0] op_sel_hi:[1,0,1] neg_lo:[0,1,0]\n"
    "v_pk_fma_f32 v[152:153], v[120:121], v[46:47], v[152:153] op_sel:[1,1,0] op_sel_hi:[1,0,1] neg_lo:[0,1,0]\n"
    "v_pk_fma_f32 v[154:155], v[138:139], v[46:47], v[154:155] op_sel:[1,1,0] op_sel_hi:[1,0,1] neg_lo:[0,1,0]\n"
    "v_pk_fma_f32 v[156:157], v[140:141], v[46:47], v[156:157] op_sel:[1,1,0] op_sel_hi:[1,0,1] neg_lo:[0,1,0]\n"
    "v_pk_fma_f32 v[158:159], v[142:143], v[46:47], v[158:159] op_sel:[1,1,0] op_sel_hi:[1,0,1] neg_lo:[0,1,0]\n"
    "v_pk_fma_f32 v[160:161], v[144:145], v[46:47], v[160:161] op_sel:[1,1,0] op_sel_hi:[1,0,1] neg_lo:[0,1,0]\n"
    "v_pk_fma_f32 v[162:163], v[146:147], v[46:47], v[162:163] op_sel:[1,1,0] op_sel_hi:[1,0,1] neg_lo:[0,1,0]\n"
    "v_pk_fma_f32 v[164:165], v[148:149], v[46:47], v[164:165] op_sel:[1,1,0] op_sel_hi:[1,0,1] neg_lo:[0,1,0]\n"
    "v_pk_mul_f32 v[174:175], v[170:171], v[182:183] op_sel:[0,0] op_sel_hi:[0,1]\n"
    "v_pk_fma_f32 v[174:175], v[170:171], v[182:183], v[174:175] op_sel:[1,1,0] op_sel_hi:[1,0,1] neg_lo:[0,1,0]\n"
    "v_cvt_pk_bf16_f32 v28, v152, v153\n"
    "v_cvt_pk_bf16_f32 v29, v154, v155\n"
    "v_cvt_pk_bf16_f32 v30, v156, v157\n"
    "v_cvt_pk_bf16_f32 v31, v158, v159\n"
    "v_cvt_pk_bf16_f32 v36, v160, v161\n"
    "v_cvt_pk_bf16_f32 v37, v162, v163\n"
    "v_cvt_pk_bf16_f32 v38, v164, v165\n"
    "v_cvt_pk_bf16_f32 v39, v170, v171\n"
    "v_cvt_pk_bf16_f32 v40, v172, v173\n"
    "v_cvt_pk_bf16_f32 v41, v174, v175\n"
    "ds_write_addtid_b32 v28 offset:32992\n"
    "ds_write_addtid_b32 v29 offset:33264\n"
    "ds_write_addtid_b32 v30 offset:33536\n"
    "ds_write_addtid_b32 v31 offset:33808\n"
    "ds_write_addtid_b32 v36 offset:34080\n"
    "ds_write_addtid_b32 v37 offset:34352\n"
    "ds_write_addtid_b32 v38 offset:34624\n"
    "ds_write_addtid_b32 v39 offset:22112\n"
    "ds_write_addtid_b32 v40 offset:22384\n"
    "ds_write_addtid_b32 v41 offset:22656\n"
    "; ---- sum over modes (MFMA)\n"
    "ds_read_b128 v[28:31], v56 offset:20480\n"
    "ds_read_b128 v[146:149], v56 offset:26192\n"
    "ds_read_b128 v[36:39], v56 offset:20512\n"
    "ds_read_b128 v[150:153], v56 offset:26224\n"
    "ds_read_b128 v[40:43], v56 offset:20544\n"
    "ds_read_b128 v[154:157], v56 offset:26256\n"
    "ds_read_b128 v[44:47], v56 offset:20576\n"
    "ds_read_b128 v[158:161], v56 offset:26288\n"
    "s_and_b32 s56, s51, 1\n"
    "s_mul_i32 s55, s56, 0xa000\n"
    "s_lshl2_add_u32 s55, s73, s55\n"
    "v_add_u32 v57, s55, v57\n"
    "s_waitcnt lgkmcnt(6)\n"
    "v_mfma_f32_32x32x16_bf16 v[178:193], v[28:31], v[146:149], 0\n"
    "ds_read_b128 v[88:91], v56 offset:20608\n"
    "ds_read_b128 v[162:165], v56 offset:26320\n"
    "v_sub_u32 v120, 0x6ff0, v1\n"
    "v_add_u32 v121, 0x14000, v120\n"
    "v_bfe_u32 v8, v0, 2, 2\n"
    "s_waitcnt lgkmcnt(6)\n"
    "s_nop 0\n"
    "v_mfma_f32_32x32x16_bf16 v[178:193], v[36:39], v[150:153], v[178:193]\n"
    "ds_read_b128 v[92:95], v56 offset:20640\n"
    "ds_read_b128 v[166:169], v56 offset:26352\n"
    "v_mul_u32_u24 v194, 0xa000, v8\n"
    "v_and_b32 v8, 1, v8\n"
    "v_lshl_add_u32 v194, v8, 7, v194\n"
    "s_waitcnt lgkmcnt(6)\n"
    "v_mfma_f32_32x32x16_bf16 v[178:193], v[40:43], v[154:157], v[178:193]\n"
    "ds_read_b128 v[138:141], v56 offset:20672\n"
    "ds_read_b128 v[170:173], v56 offset:26384\n"
    "v_bfe_u32 v8, v0, 1, 1\n"
    "s_movk_i32 s55, 0x1040\n"
    "v_mad_u32_u24 v194, v8, s55, v194\n"
    "s_waitcnt lgkmcnt(6)\n"
    "v_mfma_f32_32x32x16_bf16 v[178:193], v[44:47], v[158:161], v[178:193]\n"
    "ds_read_b128 v[142:145], v56 offset:20704\n"
    "ds_read_b128 v[174:177], v56 offset:26416\n"
    "v_mul_u32_u24 v195, 0xa080, v8\n"
    "v_and_b32 v8, 1, v0\n"
    "v_lshl_add_u32 v194, v8, 4, v194\n"
    "s_waitcnt lgkmcnt(6)\n"
    "v_mfma_f32_32x32x16_bf16 v[178:193], v[88:91], v[162:165], v[178:193]\n"
    "v_mad_u32_u24 v195, v8, s55, v195\n"
    "v_bfe_u32 v8, v4, 2, 2\n"
    "v_lshlrev_b32 v9, 1, v5\n"
    "v_sub_u32 v8, v9, v8\n"
    "v_lshl_add_u32 v195, v8, 3, v195\n"
    "s_lshl_b32 s56, s56, 6\n"
    "s_waitcnt lgkmcnt(4)\n"
    "v_mfma_f32_32x32x16_bf16 v[178:193], v[92:95], v[166:169], v[178:193]\n"
    "s_mul_i32 s55, s73, 0x14000\n"
    "s_sub_u32 s55, s55, s56\n"
    "s_add_u32 s55, s55, 0x6058\n"
    "v_add_u32 v195, s55, v195\n"
    "v_lshl_add_u32 v8, v6, 1, v5\n"
    "s_mul_i32 s55, s73, 0xa000\n"
    "s_waitcnt lgkmcnt(2)\n"
    "v_mfma_f32_32x32x16_bf16 v[178:193], v[138:141], v[170:173], v[178:193]\n"
    "s_sub_u32 s55, s55, s56\n"
    "s_add_u32 s55, s55, 0x1cb00\n"
    "v_lshl_add_u32 v196, v8, 4, s55\n"
    "v_lshlrev_b32_e64 v197, 3, v0\n"
    "v_add_u32 v197, 0x1cca0, v197\n"
    "v_and_b32 v72, s83, v68\n"
    "s_waitcnt lgkmcnt(0)\n"
    "v_mfma_f32_32x32x16_bf16 v[178:193], v[142:145], v[174:177], v[178:193]\n"
    "v_and_b32 v73, s83, v69\n"
    "v_and_b32 v74, s83, v70\n"
    "v_and_b32 v75, s83, v71\n"
    "s_waitcnt vmcnt(11)\n"
    "v_lshlrev_b32_e64 v198, 16, v76\n"
    "v_and_b32 v199, 0xffff0000, v76\n"
    "s_waitcnt vmcnt(10)\n"
    "v_lshlrev_b32 v200, 16, v77\n"
    "v_and_b32 v201, 0xffff0000, v77\n"
    "v_mul_f32 v198, 0x71800000, v198\n"
    "v_mul_f32 v199, 0x71800000, v199\n"
    "v_mul_f32 v200, 0x71800000, v200\n"
    "v_mul_f32 v201, 0x71800000, v201\n"
    "ds_write_b32 v57, v178 offset:35520\n"
    "ds_write_b32 v57, v179 offset:35776\n"
    "ds_write_b32 v57, v180 offset:36032\n"
    "ds_write_b32 v57, v181 offset:36288\n"
    "ds_write_b32 v57, v182 offset:37568\n"
    "ds_write_b32 v57, v183 offset:37824\n"
    "ds_write_b32 v57, v184 offset:38080\n"
    "ds_write_b32 v57, v185 offset:38336\n"
    "ds_write_b32 v57, v186 offset:39616\n"
    "s_mov_b32 exec_hi, 0\n"
    "ds_write_b32 v57, v187 offset:39872\n"
    "ds_write_b32 v57, v188 offset:40128\n"
    "ds_write_b32 v57, v189 offset:40384\n"
    "s_mov_b32 exec_hi, -1\n"
    "buffer_load_dwordx4 v[112:115], v2, s[36:39], s76 offen offset:0 sc1\n"
    "buffer_load_dwordx4 v[116:119], v2, s[36:39], s77 offen offset:0 sc1\n"
    "s_waitcnt lgkmcnt(0)\n"
    "s_barrier\n"
    "; ---- shifted copies of K\n"
    "ds_read_b64 v[28:29], v59\n"
    "ds_read_b64 v[92:93], v59 offset:40960\n"
    "ds_read_b64 v[30:31], v78\n"
    "ds_read_b64 v[94:95], v78 offset:40960\n"
    "ds_read_b64 v[36:37], v79\n"
    "ds_read_b64 v[138:139], v79 offset:40960\n"
    "ds_read_b64 v[38:39], v80\n"
    "ds_read_b64 v[140:141], v80 offset:40960\n"
    "ds_read_b64 v[40:41], v81\n"
    "ds_read_b64 v[142:143], v81 offset:40960\n"
    "ds_read_b64 v[42:43], v82\n"
    "ds_read_b64 v[144:145], v82 offset:40960\n"
    "ds_read_b64 v[44:45], v83\n"
    "ds_read_b64 v[146:147], v83 offset:40960\n"
    "ds_read_b64 v[46:47], v84\n"
    "ds_read_b64 v[148:149], v84 offset:40960\n"
    "ds_read_b64 v[56:57], v85\n"
    "ds_read_b64 v[150:151], v85 offset:40960\n"
    "ds_read_b64 v[88:89], v86\n"
    "ds_read_b64 v[152:153], v86 offset:40960\n"
    "ds_read_b64 v[90:91], v87\n"
    "ds_read_b64 v[154:155], v87 offset:40960\n"
    "ds_write_b128 v194, v[50:53] offset:28672\n"
    "v_pk_fma_f32 v[96:97], v[122:123], v[198:199], v[96:97] op_sel:[0,0,0] op_sel_hi:[1,0,1]\n"
    "v_pk_fma_f32 v[98:99], v[124:125], v[198:199], v[98:99] op_sel:[0,0,0] op_sel_hi:[1,0,1]\n"
    "v_pk_fma_f32 v[100:101], v[126:127], v[198:199], v[100:101] op_sel:[0,1,0] op_sel_hi:[1,1,1]\n"
    "v_pk_fma_f32 v[102:103], v[128:129], v[198:199], v[102:103] op_sel:[0,1,0] op_sel_hi:[1,1,1]\n"
    "v_pk_fma_f32 v[104:105], v[130:131], v[200:201], v[104:105] op_sel:[0,0,0] op_sel_hi:[1,0,1]\n"
    "v_pk_fma_f32 v[106:107], v[132:133], v[200:201], v[106:107] op_sel:[0,0,0] op_sel_hi:[1,0,1]\n"
    "v_pk_fma_f32 v[108:109], v[134:135], v[200:201], v[108:109] op_sel:[0,1,0] op_sel_hi:[1,1,1]\n"
    "v_pk_fma_f32 v[110:111], v[136:137], v[200:201], v[110:111] op_sel:[0,1,0] op_sel_hi:[1,1,1]\n"
    "s_waitcnt lgkmcnt(15)\n"
    "v_pk_add_f32 v[28:29], v[28:29], v[92:93]\n"
    "v_pk_add_f32 v[30:31], v[30:31], v[94:95]\n"
    "v_pk_add_f32 v[36:37], v[36:37], v[138:139]\n"
    "v_pk_add_f32 v[38:39], v[38:39], v[140:141]\n"
    "s_waitcnt lgkmcnt(13)\n"
    "v_pk_add_f32 v[40:41], v[40:41], v[142:143]\n"
    "s_waitcnt lgkmcnt(11)\n"
    "v_pk_add_f32 v[42:43], v[42:43], v[144:145]\n"
    "s_waitcnt lgkmcnt(9)\n"
    "v_pk_add_f32 v[44:45], v[44:45], v[146:147]\n"
    "s_waitcnt lgkmcnt(7)\n"
    "v_pk_add_f32 v[46:47], v[46:47], v[148:149]\n"
    "s_waitcnt lgkmcnt(5)\n"
    "v_pk_add_f32 v[56:57], v[56:57], v[150:151]\n"
    "s_waitcnt lgkmcnt(3)\n"
    "v_pk_add_f32 v[88:89], v[88:89], v[152:153]\n"
    "s_waitcnt lgkmcnt(1)\n"
    "v_pk_add_f32 v[90:91], v[90:91], v[154:155]\n"
    "v_cvt_pk_bf16_f32 v60, v28, v30\n"
    "v_cvt_pk_bf16_f32 v61, v36, v38\n"
    "v_cvt_pk_bf16_f32 v62, v40, v42\n"
    "v_cvt_pk_bf16_f32 v63, v44, v46\n"
    "ds_write_b128 v120, v[60:63] offset:45248\n"
    "v_cvt_pk_bf16_f32 v64, v29, v31\n"
    "v_cvt_pk_bf16_f32 v65, v37, v39\n"
    "v_cvt_pk_bf16_f32 v66, v41, v43\n"
    "v_cvt_pk_bf16_f32 v67, v45, v47\n"
    "ds_write_b128 v121, v[64:67] offset:45248\n"
    "v_cvt_pk_bf16_f32 v122, v30, v36\n"
    "v_cvt_pk_bf16_f32 v123, v38, v40\n"
    "v_cvt_pk_bf16_f32 v124, v42, v44\n"
    "v_cvt_pk_bf16_f32 v125, v46, v56\n"
    "ds_write_b128 v120, v[122:125] offset:41088\n"
    "v_cvt_pk_bf16_f32 v126, v31, v37\n"
    "v_cvt_pk_bf16_f32 v127, v39, v41\n"
    "v_cvt_pk_bf16_f32 v128, v43, v45\n"
    "v_cvt_pk_bf16_f32 v129, v47, v57\n"
    "ds_write_b128 v121, v[126:129] offset:41088\n"
    "v_cvt_pk_bf16_f32 v60, v36, v38\n"
    "v_cvt_pk_bf16_f32 v61, v40, v42\n"
    "v_cvt_pk_bf16_f32 v62, v44, v46\n"
    "v_cvt_pk_bf16_f32 v63, v56, v88\n"
    "ds_write_b128 v120, v[60:63] offset:4160\n"
    "v_cvt_pk_bf16_f32 v64, v37, v39\n"
    "v_cvt_pk_bf16_f32 v65, v41, v43\n"
    "v_cvt_pk_bf16_f32 v66, v45, v47\n"
    "v_cvt_pk_bf16_f32 v67, v57, v89\n"
    "ds_write_b128 v121, v[64:67] offset:4160\n"
    "v_cvt_pk_bf16_f32 v122, v38, v40\n"
    "v_cvt_pk_bf16_f32 v123, v42, v44\n"
    "v_cvt_pk_bf16_f32 v124, v46, v56\n"
    "v_cvt_pk_bf16_f32 v125, v88, v90\n"
    "ds_write_b128 v120, v[122:125] offset:0\n"
    "v_cvt_pk_bf16_f32 v126, v39, v41\n"
    "v_cvt_pk_bf16_f32 v127, v43, v45\n"
    "v_cvt_pk_bf16_f32 v128, v47, v57\n"
    "v_cvt_pk_bf16_f32 v129, v89, v91\n"
    "ds_write_b128 v121, v[126:129] offset:0\n"
    "; ---- U checked and staged\n"
    "s_waitcnt vmcnt(0)\n"
    "v_xor_b32 v112, v72, v112\n"
    "v_xor_b32 v113, v73, v113\n"
    "v_xor_b32 v114, v74, v114\n"
    "v_xor_b32 v115, v75, v115\n"
    "v_xor_b32 v116, v72, v116\n"
    "v_xor_b32 v117, v73, v117\n"
    "v_xor_b32 v118, v74, v118\n"
    "v_xor_b32_e64 v119, v75, v119\n"
    "v_or3_b32 v8, v112, v113, v114\n"
    "v_or3_b32 v9, v116, v117, v118\n"
    "v_or3_b32 v8, v8, v115, v119\n"
    "v_bitop3_b32 v8, v8, v9, s83 bitop3:0xa8\n"
    "v_cmp_ne_u32_e64 vcc, 0, v8\n"
    "s_nop 0\n"
    "s_cbranch_vccnz .Lk_slow_1\n"
    ".Lk_back_2:\n"
    "ds_write_b64 v197, v[112:113] offset:0\n"
    "ds_write_b64 v197, v[114:115] offset:40960\n"
    "ds_write_b64 v197, v[116:117] offset:2048\n"
    "ds_write_b64 v197, v[118:119] offset:43008\n"
    "s_waitcnt lgkmcnt(0)\n"
    "s_barrier\n"
    "; ---- convolution\n"
    "ds_read_b64 v[28:29], v195 offset:3968\n"
    "ds_read_b64 v[30:31], v195 offset:3976\n"
    "ds_read_b128 v[124:127], v196 offset:384\n"
    "ds_read_b64 v[36:37], v195 offset:3456\n"
    "ds_read_b64 v[38:39], v195 offset:3464\n"
    "ds_read_b128 v[128:131], v196 offset:896\n"
    "ds_read_b64 v[40:41], v195 offset:2944\n"
    "ds_read_b64 v[42:43], v195 offset:2952\n"
    "ds_read_b128 v[132:135], v196 offset:1408\n"
    "ds_read_b64 v[44:45], v195 offset:2432\n"
    "ds_read_b64 v[46:47], v195 offset:2440\n"
    "ds_read_b128 v[136:139], v196 offset:1920\n"
    "s_waitcnt lgkmcnt(9)\n"
    "s_nop 0\n"
    "v_mfma_f32_16x16x32_bf16 v[188:191], v[28:31], v[124:127], 0\n"
    "ds_read_b64 v[50:51], v195 offset:1920\n"
    "s_add_u32 m0, s62, 0x2000\n"
    "s_add_u32 s57, s63, 0x38000\n"
    "s_add_u32 s72, s63, 0xffe78000\n"
    "s_min_u32 s57, s57, s72\n"
    "buffer_load_dwordx4 v12, s[40:43], s57 offen offset:0 lds\n"
    "s_waitcnt lgkmcnt(7)\n"
    "v_mfma_f32_16x16x32_bf16 v[198:201], v[28:31], v[128:131], 0\n"
    "ds_read_b64 v[52:53], v195 offset:1928\n"
    "s_lshl_b32 s55, s51, 18\n"
    "v_mfma_f32_16x16x32_bf16 v[198:201], v[36:39], v[124:127], v[198:201]\n"
    "ds_read_b128 v[140:143], v196 offset:2432\n"
    "s_lshl_b32 s56, s50, 10\n"
    "s_waitcnt lgkmcnt(6)\n"
    "v_mfma_f32_16x16x32_bf16 v[202:205], v[28:31], v[132:135], 0\n"
    "ds_read_b64 v[60:61], v195 offset:1408\n"
    "s_add_u32 s55, s55, s56\n"
    "v_mfma_f32_16x16x32_bf16 v[202:205], v[36:39], v[128:131], v[202:205]\n"
    "ds_read_b64 v[62:63], v195 offset:1416\n"
    "buffer_load_dwordx4 v13, s[40:43], s57 offen offset:1024 lds\n"
    "v_mfma_f32_16x16x32_bf16 v[202:205], v[40:43], v[124:127], v[202:205]\n"
    "ds_read_b128 v[144:147], v196 offset:2944\n"
    "s_add_u32 s78, s55, 0x520000\n"
    "s_waitcnt lgkmcnt(6)\n"
    "v_mfma_f32_16x16x32_bf16 v[206:209], v[28:31], v[136:139], 0\n"
    "ds_read_b64 v[64:65], v195 offset:896\n"
    "s_add_u32 s79, s78, 0x100000\n"
    "v_mfma_f32_16x16x32_bf16 v[206:209], v[36:39], v[132:135], v[206:209]\n"
    "ds_read_b64 v[66:67], v195 offset:904\n"
    "s_mul_i32 s55, s58, 0x38000\n"
    "v_mfma_f32_16x16x32_bf16 v[206:209], v[40:43], v[128:131], v[206:209]\n"
    "ds_read_b128 v[148:151], v196 offset:3456\n"
    "buffer_load_dwordx4 v14, s[40:43], s57 offen offset:2048 lds\n"
    "v_mfma_f32_16x16x32_bf16 v[206:209], v[44:47], v[124:127], v[206:209]\n"
    "ds_read_b64 v[72:73], v195 offset:384\n"
    "s_lshl2_add_u32 s56, s59, s51\n"
    "s_waitcnt lgkmcnt(7)\n"
    "v_mfma_f32_16x16x32_bf16 v[210:213], v[28:31], v[140:143], 0\n"
    "ds_read_b64 v[74:75], v195 offset:392\n"
    "s_lshl_b32 s56, s56, 11\n"
    "v_mfma_f32_16x16x32_bf16 v[210:213], v[36:39], v[136:139], v[210:213]\n"
    "ds_read_b128 v[152:155], v196 offset:3968\n"
    "s_add_u32 s55, s55, s56\n"
    "v_mfma_f32_16x16x32_bf16 v[210:213], v[40:43], v[132:135], v[210:213]\n"
    "ds_read_b64 v[76:77], v195 offset:3840\n"
    "buffer_load_dwordx4 v15, s[40:43], s57 offen offset:3072 lds\n"
    "v_mfma_f32_16x16x32_bf16 v[210:213], v[44:47], v[128:131], v[210:213]\n"
    "ds_read_b64 v[78:79], v195 offset:3848\n"
    "s_add_u32 s80, s55, 0x720000\n"
    "v_mfma_f32_16x16x32_bf16 v[210:213], v[50:53], v[124:127], v[210:213]\n"
    "ds_read_b128 v[156:159], v196 offset:256\n"
    "v_and_b32 v8, 0x200, v3\n"
    "s_waitcnt lgkmcnt(9)\n"
    "v_mfma_f32_16x16x32_bf16 v[214:217], v[28:31], v[144:147], 0\n"
    "ds_read_b64 v[80:81], v195 offset:3328\n"
    "v_add3_u32 v24, v3, v8, s61\n"
    "v_mfma_f32_16x16x32_bf16 v[214:217], v[36:39], v[140:143], v[214:217]\n"
    "ds_read_b64 v[82:83], v195 offset:3336\n"
    "s_add_u32 m0, s62, 0x3000\n"
    "s_add_u32 s57, s63, 0x70000\n"
    "s_add_u32 s72, s63, 0xffeb0000\n"
    "s_min_u32 s57, s57, s72\n"
    "buffer_load_dwordx4 v12, s[40:43], s57 offen offset:0 lds\n"
    "v_mfma_f32_16x16x32_bf16 v[214:217], v[40:43], v[136:139], v[214:217]\n"
    "ds_read_b128 v[160:163], v196 offset:768\n"
    "s_lshl_b32 s55, s50, 6\n"
    "v_mfma_f32_16x16x32_bf16 v[214:217], v[44:47], v[132:135], v[214:217]\n"
    "ds_read_b64 v[84:85], v195 offset:2816\n"
    "s_add_u32 s81, s55, 0x30000\n"
    "v_mfma_f32_16x16x32_bf16 v[214:217], v[50:53], v[128:131], v[214:217]\n"
    "ds_read_b64 v[86:87], v195 offset:2824\n"
    "s_min_u32 s55, s51, 1\n"
    "v_mfma_f32_16x16x32_bf16 v[214:217], v[60:63], v[124:127], v[214:217]\n"
    "ds_read_b128 v[164:167], v196 offset:1280\n"
    "buffer_load_dwordx4 v13, s[40:43], s57 offen offset:1024 lds\n"
    "s_waitcnt lgkmcnt(12)\n"
    "v_mfma_f32_16x16x32_bf16 v[218:221], v[28:31], v[148:151], 0\n"
    "ds_read_b64 v[88:89], v195 offset:2304\n"
    "s_lshl_b32 s55, s55, 30\n"
    "v_mfma_f32_16x16x32_bf16 v[218:221], v[36:39], v[144:147], v[218:221]\n"
    "ds_read_b64 v[90:91], v195 offset:2312\n"
    "v_lshl_add_u32 v58, v55, 3, s55\n"
    "v_mfma_f32_16x16x32_bf16 v[218:221], v[40:43], v[140:143], v[218:221]\n"
    "ds_read_b128 v[168:171], v196 offset:1792\n"
    "v_and_b32 v8, 14, v4\n"
    "v_mfma_f32_16x16x32_bf16 v[218:221], v[44:47], v[136:139], v[218:221]\n"
    "ds_read_b64 v[92:93], v195 offset:1792\n"
    "buffer_load_dwordx4 v14, s[40:43], s57 offen offset:2048 lds\n"
    "v_mfma_f32_16x16x32_bf16 v[218:221], v[50:53], v[132:135], v[218:221]\n"
    "ds_read_b64 v[94:95], v195 offset:1800\n"
    "v_lshl_add_u32 v58, v8, 24, v58\n"
    "v_mfma_f32_16x16x32_bf16 v[218:221], v[60:63], v[128:131], v[218:221]\n"
    "ds_read_b128 v[172:175], v196 offset:2304\n"
    "s_lshr_b32 s55, s26, 7\n"
    "v_mfma_f32_16x16x32_bf16 v[218:221], v[64:67], v[124:127], v[218:221]\n"
    "ds_read_b64 v[112:113], v195 offset:1280\n"
    "s_sub_u32 s55, s65, s55\n"
    "s_waitcnt lgkmcnt(15)\n"
    "v_mfma_f32_16x16x32_bf16 v[222:225], v[28:31], v[152:155], 0\n"
    "ds_read_b64 v[114:115], v195 offset:1288\n"
    "buffer_store_dwordx4 v[32:35], v3, s[36:39], s78 offen offset:0 sc1\n"
    "v_mfma_f32_16x16x32_bf16 v[222:225], v[36:39], v[148:151], v[222:225]\n"
    "ds_read_b128 v[176:179], v196 offset:2816\n"
    "s_add_u32 s55, s55, 4\n"
    "v_mfma_f32_16x16x32_bf16 v[222:225], v[40:43], v[144:147], v[222:225]\n"
    "ds_read_b64 v[116:117], v195 offset:768\n"
    "s_and_b32 s55, s55, 7\n"
    "v_mfma_f32_16x16x32_bf16 v[222:225], v[44:47], v[140:143], v[222:225]\n"
    "ds_read_b64 v[118:119], v195 offset:776\n"
    "s_lshr_b32 s56, s53, 1\n"
    "v_mfma_f32_16x16x32_bf16 v[222:225], v[50:53], v[136:139], v[222:225]\n"
    "ds_read_b128 v[180:183], v196 offset:3328\n"
    "buffer_store_dwordx4 v[32:35], v3, s[36:39], s79 offen offset:0 sc1\n"
    "v_mfma_f32_16x16x32_bf16 v[222:225], v[60:63], v[132:135], v[222:225]\n"
    "ds_read_b64 v[120:121], v195 offset:256\n"
    "s_lshl3_add_u32 s60, s56, s55\n"
    "v_mfma_f32_16x16x32_bf16 v[222:225], v[64:67], v[128:131], v[222:225]\n"
    "ds_read_b64 v[122:123], v195 offset:264\n"
    "s_mov_b32 s44, s26\n"
    "v_mfma_f32_16x16x32_bf16 v[222:225], v[72:75], v[124:127], v[222:225]\n"
    "ds_read_b128 v[184:187], v196 offset:3840\n"
    "s_and_b32 s45, s27, 0xffff\n"
    "s_waitcnt lgkmcnt(15)\n"
    "v_mfma_f32_16x16x32_bf16 v[188:191], v[76:79], v[156:159], v[188:191]\n"
    "ds_read_b64 v[28:29], v195 offset:3712\n"
    "buffer_store_dwordx2 v[32:33], v58, s[36:39], s81 offen offset:0 sc1\n"
    "v_mfma_f32_16x16x32_bf16 v[198:201], v[76:79], v[160:163], v[198:201]\n"
    "ds_read_b64 v[30:31], v195 offset:3720\n"
    "s_mov_b32 s46, 0x700000\n"
    "v_mfma_f32_16x16x32_bf16 v[198:201], v[80:83], v[156:159], v[198:201]\n"
    "ds_read_b128 v[124:127], v196 offset:128\n"
    "s_mov_b32 s47, s39\n"
    "v_mfma_f32_16x16x32_bf16 v[202:205], v[76:79], v[164:167], v[202:205]\n"
    "ds_read_b64 v[36:37], v195 offset:3200\n"
    "s_mul_i32 s55, s51, 0x1c0000\n"
    "v_mfma_f32_16x16x32_bf16 v[202:205], v[80:83], v[160:163], v[202:205]\n"
    "ds_read_b64 v[38:39], v195 offset:3208\n"
    "buffer_load_dwordx4 v15, s[40:43], s57 offen offset:3072 lds\n"
    "v_mfma_f32_16x16x32_bf16 v[202:205], v[84:87], v[156:159], v[202:205]\n"
    "ds_read_b128 v[128:131], v196 offset:640\n"
    "s_lshl_b32 s56, s60, 7\n"
    "s_waitcnt lgkmcnt(15)\n"
    "v_mfma_f32_16x16x32_bf16 v[206:209], v[76:79], v[168:171], v[206:209]\n"
    "ds_read_b64 v[40:41], v195 offset:2688\n"
    "s_add_u32 s55, s55, s56\n"
    "v_mfma_f32_16x16x32_bf16 v[206:209], v[80:83], v[164:167], v[206:209]\n"
    "ds_read_b64 v[42:43], v195 offset:2696\n"
    "v_add_u32 v16, s55, v16\n"
    "v_mfma_f32_16x16x32_bf16 v[206:209], v[84:87], v[160:163], v[206:209]\n"
    "ds_read_b128 v[132:135], v196 offset:1152\n"
    "s_add_u32 m0, s62, 0x4000\n"
    "s_add_u32 s57, s63, 0xa8000\n"
    "s_add_u32 s72, s63, 0xffee8000\n"
    "s_min_u32 s57, s57, s72\n"
    "buffer_load_dwordx4 v12, s[40:43], s57 offen offset:0 lds\n"
    "v_mfma_f32_16x16x32_bf16 v[206:209], v[88:91], v[156:159], v[206:209]\n"
    "ds_read_b64 v[44:45], v195 offset:2176\n"
    "v_xor_b32 v17, 64, v16\n"
    "v_mfma_f32_16x16x32_bf16 v[210:213], v[76:79], v[172:175], v[210:213]\n"
    "ds_read_b64 v[46:47], v195 offset:2184\n"
    "v_add_u32 v18, 0xf800, v16\n"
    "v_mfma_f32_16x16x32_bf16 v[210:213], v[80:83], v[168:171], v[210:213]\n"
    "ds_read_b128 v[136:139], v196 offset:1664\n"
    "v_add_u32 v19, 0x17400, v17\n"
    "v_mfma_f32_16x16x32_bf16 v[210:213], v[84:87], v[164:167], v[210:213]\n"
    "ds_read_b64 v[50:51], v195 offset:1664\n"
    "buffer_load_dwordx4 v13, s[40:43], s57 offen offset:1024 lds\n"
    "v_mfma_f32_16x16x32_bf16 v[210:213], v[88:91], v[160:163], v[210:213]\n"
    "ds_read_b64 v[52:53], v195 offset:1672\n"
    "v_add_u32 v17, 0x7c00, v17\n"
    "v_mfma_f32_16x16x32_bf16 v[210:213], v[92:95], v[156:159], v[210:213]\n"
    "ds_read_b128 v[140:143], v196 offset:2176\n"
    "s_min_u32 s55, s70, 6\n"
    "s_waitcnt lgkmcnt(15)\n"
    "v_mfma_f32_16x16x32_bf16 v[214:217], v[76:79], v[176:179], v[214:217]\n"
    "ds_read_b64 v[60:61], v195 offset:1152\n"
    "s_lshl_b32 s64, s55, 18\n"
    "v_mfma_f32_16x16x32_bf16 v[214:217], v[80:83], v[172:175], v[214:217]\n"
    "ds_read_b64 v[62:63], v195 offset:1160\n"
    "buffer_load_dwordx4 v14, s[40:43], s57 offen offset:2048 lds\n"
    "v_mfma_f32_16x16x32_bf16 v[214:217], v[84:87], v[168:171], v[214:217]\n"
    "ds_read_b128 v[144:147], v196 offset:2688\n"
    "v_bfe_u32 v8, v4, 2, 2\n"
    "v_mfma_f32_16x16x32_bf16 v[214:217], v[88:91], v[164:167], v[214:217]\n"
    "ds_read_b64 v[64:65], v195 offset:640\n"
    "v_lshl_add_u32 v9, v5, 3, v8\n"
    "v_mfma_f32_16x16x32_bf16 v[214:217], v[92:95], v[160:163], v[214:217]\n"
    "ds_read_b64 v[66:67], v195 offset:648\n"
    "v_lshlrev_b32 v9, 7, v9\n"
    "v_mfma_f32_16x16x32_bf16 v[214:217], v[112:115], v[156:159], v[214:217]\n"
    "ds_read_b128 v[148:151], v196 offset:3200\n"
    "buffer_load_dwordx4 v15, s[40:43], s57 offen offset:3072 lds\n"
    "v_mfma_f32_16x16x32_bf16 v[218:221], v[76:79], v[180:183], v[218:221]\n"
    "ds_read_b64 v[72:73], v195 offset:128\n"
    "v_and_b32_e64 v10, 3, v4\n"
    "v_mfma_f32_16x16x32_bf16 v[218:221], v[80:83], v[176:179], v[218:221]\n"
    "ds_read_b64 v[74:75], v195 offset:136\n"
    "v_lshl_add_u32 v9, v10, 3, v9\n"
    "v_mfma_f32_16x16x32_bf16 v[218:221], v[84:87], v[172:175], v[218:221]\n"
    "ds_read_b128 v[152:155], v196 offset:3712\n"
    "v_add_u32_e64 v9, s62, v9\n"
    "v_mfma_f32_16x16x32_bf16 v[218:221], v[88:91], v[168:171], v[218:221]\n"
    "v_bfe_u32 v8, v4, 3, 2\n"
    "v_mfma_f32_16x16x32_bf16 v[218:221], v[92:95], v[164:167], v[218:221]\n"
    "s_add_u32 m0, s62, 0x5000\n"
    "s_add_u32 s57, s63, 0xe0000\n"
    "s_add_u32 s72, s63, 0xfff20000\n"
    "s_min_u32 s57, s57, s72\n"
    "buffer_load_dwordx4 v12, s[40:43], s57 offen offset:0 lds\n"
    "v_mfma_f32_16x16x32_bf16 v[218:221], v[112:115], v[160:163], v[218:221]\n"
    "s_add_u32 s55, s51, 0\n"
    "v_mfma_f32_16x16x32_bf16 v[218:221], v[116:119], v[156:159], v[218:221]\n"
    "s_and_b32 s55, s55, 3\n"
    "v_mfma_f32_16x16x32_bf16 v[222:225], v[76:79], v[184:187], v[222:225]\n"
    "v_xor_b32 v10, s55, v8\n"
    "v_mfma_f32_16x16x32_bf16 v[222:225], v[80:83], v[180:183], v[222:225]\n"
    "v_lshl_add_u32 v20, v10, 5, v9\n"
    "v_mfma_f32_16x16x32_bf16 v[222:225], v[84:87], v[176:179], v[222:225]\n"
    "buffer_load_dwordx4 v13, s[40:43], s57 offen offset:1024 lds\n"
    "v_mfma_f32_16x16x32_bf16 v[222:225], v[88:91], v[172:175], v[222:225]\n"
    "s_add_u32 s55, s51, 1\n"
    "v_mfma_f32_16x16x32_bf16 v[222:225], v[92:95], v[168:171], v[222:225]\n"
    "s_and_b32 s55, s55, 3\n"
    "v_mfma_f32_16x16x32_bf16 v[222:225], v[112:115], v[164:167], v[222:225]\n"
    "v_xor_b32_e64 v10, s55, v8\n"
    "v_mfma_f32_16x16x32_bf16 v[222:225], v[116:119], v[160:163], v[222:225]\n"
    "v_lshl_add_u32 v21, v10, 5, v9\n"
    "v_mfma_f32_16x16x32_bf16 v[222:225], v[120:123], v[156:159], v[222:225]\n"
    "buffer_load_dwordx4 v14, s[40:43], s57 offen offset:2048 lds\n"
    "s_waitcnt lgkmcnt(15)\n"
    "v_mfma_f32_16x16x32_bf16 v[188:191], v[28:31], v[124:127], v[188:191]\n"
    "ds_read_b64 v[76:77], v195 offset:3584\n"
    "s_add_u32 s55, s51, 2\n"
    "v_mfma_f32_16x16x32_bf16 v[198:201], v[28:31], v[128:131], v[198:201]\n"
    "ds_read_b64 v[78:79], v195 offset:3592\n"
    "s_and_b32 s55, s55, 3\n"
    "v_mfma_f32_16x16x32_bf16 v[198:201], v[36:39], v[124:127], v[198:201]\n"
    "ds_read_b128 v[156:159], v196 offset:0\n"
    "v_xor_b32 v10, s55, v8\n"
    "v_mfma_f32_16x16x32_bf16 v[202:205], v[28:31], v[132:135], v[202:205]\n"
    "ds_read_b64 v[80:81], v195 offset:3072\n"
    "buffer_load_dwordx4 v15, s[40:43], s57 offen offset:3072 lds\n"
    "v_mfma_f32_16x16x32_bf16 v[202:205], v[36:39], v[128:131], v[202:205]\n"
    "ds_read_b64 v[82:83], v195 offset:3080\n"
    "v_lshl_add_u32 v22, v10, 5, v9\n"
    "v_mfma_f32_16x16x32_bf16 v[202:205], v[40:43], v[124:127], v[202:205]\n"
    "ds_read_b128 v[160:163], v196 offset:512\n"
    "s_add_u32 s55, s51, 3\n"
    "s_waitcnt lgkmcnt(15)\n"
    "v_mfma_f32_16x16x32_bf16 v[206:209], v[28:31], v[136:139], v[206:209]\n"
    "ds_read_b64 v[84:85], v195 offset:2560\n"
    "s_and_b32 s55, s55, 3\n"
    "v_mfma_f32_16x16x32_bf16 v[206:209], v[36:39], v[132:135], v[206:209]\n"
    "ds_read_b64 v[86:87], v195 offset:2568\n"
    "v_xor_b32 v10, s55, v8\n"
    "v_mfma_f32_16x16x32_bf16 v[206:209], v[40:43], v[128:131], v[206:209]\n"
    "ds_read_b128 v[164:167], v196 offset:1024\n"
    "v_lshl_add_u32 v23, v10, 5, v9\n"
    "v_mfma_f32_16x16x32_bf16 v[206:209], v[44:47], v[124:127], v[206:209]\n"
    "ds_read_b64_tr_b16 a[0:1], v20 offset:0\n"
    "ds_read_b64_tr_b16 a[2:3], v20 offset:512\n"
    "v_mfma_f32_16x16x32_bf16 v[210:213], v[28:31], v[140:143], v[210:213]\n"
    "ds_read_b64_tr_b16 a[4:5], v21 offset:0\n"
    "ds_read_b64_tr_b16 a[6:7], v21 offset:512\n"
    "v_mfma_f32_16x16x32_bf16 v[210:213], v[36:39], v[136:139], v[210:213]\n"
    "ds_read_b64_tr_b16 a[8:9], v22 offset:0\n"
    "ds_read_b64_tr_b16 a[10:11], v22 offset:512\n"
    "v_mfma_f32_16x16x32_bf16 v[210:213], v[40:43], v[132:135], v[210:213]\n"
    "ds_read_b64_tr_b16 a[12:13], v23 offset:0\n"
    "ds_read_b64_tr_b16 a[14:15], v23 offset:512\n"
    "v_mfma_f32_16x16x32_bf16 v[210:213], v[44:47], v[128:131], v[210:213]\n"
    "ds_read_b64_tr_b16 a[128:129], v20 offset:4096\n"
    "s_lshl2_add_u32 s55, s59, s51\n"
    "v_mfma_f32_16x16x32_bf16 v[210:213], v[50:53], v[124:127], v[210:213]\n"
    "ds_read_b64_tr_b16 a[130:131], v20 offset:4608\n"
    "s_lshl_b32 s55, s55, 5\n"
    "s_waitcnt lgkmcnt(15)\n"
    "v_mfma_f32_16x16x32_bf16 v[214:217], v[28:31], v[144:147], v[214:217]\n"
    "ds_read_b64_tr_b16 a[132:133], v21 offset:4096\n"
    "v_lshl_add_u32 v8, v5, 3, s55\n"
    "v_mfma_f32_16x16x32_bf16 v[214:217], v[36:39], v[140:143], v[214:217]\n"
    "ds_read_b64_tr_b16 a[134:135], v21 offset:4608\n"
    "global_load_dwordx2 v[26:27], v8, s[24:25]\n"
    "v_mfma_f32_16x16x32_bf16 v[214:217], v[40:43], v[136:139], v[214:217]\n"
    "ds_read_b64_tr_b16 a[136:137], v22 offset:4096\n"
    "s_lshl2_add_u32 s55, s60, s51\n"
    "v_mfma_f32_16x16x32_bf16 v[214:217], v[44:47], v[132:135], v[214:217]\n"
    "ds_read_b64_tr_b16 a[138:139], v22 offset:4608\n"
    "s_lshl_b32 s55, s55, 5\n"
    "v_mfma_f32_16x16x32_bf16 v[214:217], v[50:53], v[128:131], v[214:217]\n"
    "ds_read_b64_tr_b16 a[140:141], v23 offset:4096\n"
    "s_waitcnt lgkmcnt(7)\n"
    "s_add_u32 m0, s62, 0\n"
    "s_mov_b32 s71, s64\n"
    "buffer_load_dwordx4 v16, s[44:47], s71 offen offset:0 lds\n"
    "v_mfma_f32_16x16x32_bf16 v[214:217], v[60:63], v[124:127], v[214:217]\n"
    "ds_read_b64_tr_b16 a[142:143], v23 offset:4608\n"
    "v_lshl_add_u32 v9, v6, 1, s55\n"
    "v_mfma_f32_16x16x32_bf16 v[218:221], v[28:31], v[148:151], v[218:221]\n"
    "ds_read_b64 v[88:89], v195 offset:2048\n"
    "v_mov_b32_e64 v25, 0\n"
    "v_mfma_f32_16x16x32_bf16 v[218:221], v[36:39], v[144:147], v[218:221]\n"
    "ds_read_b64 v[90:91], v195 offset:2056\n"
    "buffer_load_dwordx4 v17, s[44:47], s71 offen offset:1024 lds\n"
    "v_mfma_f32_16x16x32_bf16 v[218:221], v[40:43], v[140:143], v[218:221]\n"
    "ds_read_b128 v[168:171], v196 offset:1536\n"
    "global_load_short_d16_hi v25, v9, s[28:29]\n"
    "v_mfma_f32_16x16x32_bf16 v[218:221], v[44:47], v[136:139], v[218:221]\n"
    "ds_read_b64 v[92:93], v195 offset:1536\n"
    "v_lshlrev_b32_e64 v7, 4, v6\n"
    "v_mfma_f32_16x16x32_bf16 v[218:221], v[50:53], v[132:135], v[218:221]\n"
    "ds_read_b64 v[94:95], v195 offset:1544\n"
    "buffer_load_dwordx4 v18, s[44:47], s71 offen offset:2048 lds\n"
    "v_mfma_f32_16x16x32_bf16 v[218:221], v[60:63], v[128:131], v[218:221]\n"
    "ds_read_b128 v[172:175], v196 offset:2048\n"
    "v_lshl_add_u32 v7, v5, 10, v7\n"
    "v_mfma_f32_16x16x32_bf16 v[218:221], v[64:67], v[124:127], v[218:221]\n"
    "ds_read_b64 v[112:113], v195 offset:1024\n"
    "v_add_u32_e64 v56, s62, v3\n"
    "v_mfma_f32_16x16x32_bf16 v[222:225], v[28:31], v[152:155], v[222:225]\n"
    "ds_read_b64 v[114:115], v195 offset:1032\n"
    "buffer_load_dwordx4 v19, s[44:47], s71 offen offset:3072 lds\n"
    "v_mfma_f32_16x16x32_bf16 v[222:225], v[36:39], v[148:151], v[222:225]\n"
    "ds_read_b128 v[176:179], v196 offset:2560\n"
    "v_add_u32 v57, 0x7000, v1\n"
    "v_mfma_f32_16x16x32_bf16 v[222:225], v[40:43], v[144:147], v[222:225]\n"
    "ds_read_b64 v[116:117], v195 offset:512\n"
    "v_add_u32 v59, 0x14000, v57\n"
    "v_mfma_f32_16x16x32_bf16 v[222:225], v[44:47], v[140:143], v[222:225]\n"
    "ds_read_b64 v[118:119], v195 offset:520\n"
    "v_lshl_add_u32 v8, v6, 2, v5\n"
    "v_mfma_f32_16x16x32_bf16 v[222:225], v[50:53], v[136:139], v[222:225]\n"
    "ds_read_b128 v[180:183], v196 offset:3072\n"
    "s_lshl_b32 s55, s51, 10\n"
    "v_mfma_f32_16x16x32_bf16 v[222:225], v[60:63], v[132:135], v[222:225]\n"
    "ds_read_b64 v[120:121], v195 offset:0\n"
    "v_lshl_add_u32 v192, v8, 4, s55\n"
    "v_mfma_f32_16x16x32_bf16 v[222:225], v[64:67], v[128:131], v[222:225]\n"
    "ds_read_b64 v[122:123], v195 offset:8\n"
    "s_lshl_b32 s55, s50, 5\n"
    "v_mfma_f32_16x16x32_bf16 v[222:225], v[72:75], v[124:127], v[222:225]\n"
    "ds_read_b128 v[184:187], v196 offset:3584\n"
    "s_add_u32 s76, s55, 0x310000\n"
    "v_mfma_f32_16x16x32_bf16 v[188:191], v[76:79], v[156:159], v[188:191]\n"
    "s_add_u32 s77, s55, 0x414000\n"
    "v_mfma_f32_16x16x32_bf16 v[198:201], v[76:79], v[160:163], v[198:201]\n"
    "v_mfma_f32_16x16x32_bf16 v[198:201], v[80:83], v[156:159], v[198:201]\n"
    "v_mfma_f32_16x16x32_bf16 v[202:205], v[76:79], v[164:167], v[202:205]\n"
    "v_mfma_f32_16x16x32_bf16 v[202:205], v[80:83], v[160:163], v[202:205]\n"
    "v_mfma_f32_16x16x32_bf16 v[202:205], v[84:87], v[156:159], v[202:205]\n"
    "s_waitcnt lgkmcnt(12)\n"
    "v_mfma_f32_16x16x32_bf16 v[206:209], v[76:79], v[168:171], v[206:209]\n"
    "s_waitcnt lgkmcnt(0)\n"
    "s_barrier\n"
    "ds_write_b128 v56, v[188:191] offset:28672\n"
    "v_mfma_f32_16x16x32_bf16 v[206:209], v[80:83], v[164:167], v[206:209]\n"
    "v_mfma_f32_16x16x32_bf16 v[206:209], v[84:87], v[160:163], v[206:209]\n"
    "ds_write_b128 v56, v[198:201] offset:29696\n"
    "v_mfma_f32_16x16x32_bf16 v[206:209], v[88:91], v[156:159], v[206:209]\n"
    "v_mfma_f32_16x16x32_bf16 v[210:213], v[76:79], v[172:175], v[210:213]\n"
    "v_mfma_f32_16x16x32_bf16 v[210:213], v[80:83], v[168:171], v[210:213]\n"
    "ds_write_b128 v56, v[202:205] offset:30720\n"
    "v_mfma_f32_16x16x32_bf16 v[210:213], v[84:87], v[164:167], v[210:213]\n"
    "v_mfma_f32_16x16x32_bf16 v[210:213], v[88:91], v[160:163], v[210:213]\n"
    "v_mfma_f32_16x16x32_bf16 v[210:213], v[92:95], v[156:159], v[210:213]\n"
    "v_mfma_f32_16x16x32_bf16 v[214:217], v[76:79], v[176:179], v[214:217]\n"
    "s_nop 0\n"
    "ds_write_b128 v56, v[206:209] offset:31744\n"
    "v_mfma_f32_16x16x32_bf16 v[214:217], v[80:83], v[172:175], v[214:217]\n"
    "v_mfma_f32_16x16x32_bf16 v[214:217], v[84:87], v[168:171], v[214:217]\n"
    "v_mfma_f32_16x16x32_bf16 v[214:217], v[88:91], v[164:167], v[214:217]\n"
    "v_mfma_f32_16x16x32_bf16 v[214:217], v[92:95], v[160:163], v[214:217]\n"
    "v_mfma_f32_16x16x32_bf16 v[214:217], v[112:115], v[156:159], v[214:217]\n"
    "ds_write_b128 v56, v[210:213] offset:32768\n"
    "v_mfma_f32_16x16x32_bf16 v[218:221], v[76:79], v[180:183], v[218:221]\n"
    "v_mfma_f32_16x16x32_bf16 v[218:221], v[80:83], v[176:179], v[218:221]\n"
    "v_mfma_f32_16x16x32_bf16 v[218:221], v[84:87], v[172:175], v[218:221]\n"
    "v_mfma_f32_16x16x32_bf16 v[218:221], v[88:91], v[168:171], v[218:221]\n"
    "v_mfma_f32_16x16x32_bf16 v[218:221], v[92:95], v[164:167], v[218:221]\n"
    "v_mfma_f32_16x16x32_bf16 v[218:221], v[112:115], v[160:163], v[218:221]\n"
    "s_nop 0\n"
    "ds_write_b128 v56, v[214:217] offset:33792\n"
    "v_mfma_f32_16x16x32_bf16 v[218:221], v[116:119], v[156:159], v[218:221]\n"
    "v_mfma_f32_16x16x32_bf16 v[222:225], v[76:79], v[184:187], v[222:225]\n"
    "v_mfma_f32_16x16x32_bf16 v[222:225], v[80:83], v[180:183], v[222:225]\n"
    "v_mfma_f32_16x16x32_bf16 v[222:225], v[84:87], v[176:179], v[222:225]\n"
    "v_mfma_f32_16x16x32_bf16 v[222:225], v[88:91], v[172:175], v[222:225]\n"
    "v_mfma_f32_16x16x32_bf16 v[222:225], v[92:95], v[168:171], v[222:225]\n"
    "v_mfma_f32_16x16x32_bf16 v[222:225], v[112:115], v[164:167], v[222:225]\n"
    "s_nop 1\n"
    "ds_write_b128 v56, v[218:221] offset:34816\n"
    "v_mfma_f32_16x16x32_bf16 v[222:225], v[116:119], v[160:163], v[222:225]\n"
    "v_mfma_f32_16x16x32_bf16 v[222:225], v[120:123], v[156:159], v[222:225]\n"
    "s_nop 7\n"
    "ds_write_b128 v56, v[222:225] offset:35840\n"
    "s_waitcnt vmcnt(15)\n"
    "s_waitcnt lgkmcnt(0)\n"
    "s_barrier\n"
    "; ---- Y published, weights streamed\n"
    "ds_read_b128 v[28:31], v57 offset:0\n"
    "ds_read_b128 v[36:39], v57 offset:40960\n"
    "ds_read_b128 v[40:43], v59 offset:0\n"
    "ds_read_b128 v[44:47], v59 offset:40960\n"
    "ds_read_b128 v[50:53], v57 offset:4096\n"
    "ds_read_b128 v[60:63], v57 offset:45056\n"
    "ds_read_b128 v[64:67], v59 offset:4096\n"
    "ds_read_b128 v[72:75], v59 offset:45056\n"
    "s_waitcnt lgkmcnt(6)\n"
    "v_pk_add_f32 v[28:29], v[28:29], v[36:37]\n"
    "v_pk_add_f32 v[30:31], v[30:31], v[38:39]\n"
    "s_waitcnt lgkmcnt(4)\n"
    "v_pk_add_f32 v[40:41], v[40:41], v[44:45]\n"
    "v_pk_add_f32 v[42:43], v[42:43], v[46:47]\n"
    "v_cvt_pk_bf16_f32 v188, v28, v29\n"
    "v_cvt_pk_bf16_f32 v189, v30, v31\n"
    "v_cvt_pk_bf16_f32 v190, v40, v41\n"
    "v_cvt_pk_bf16_f32 v191, v42, v43\n"
    "buffer_store_dwordx4 v[188:191], v192, s[36:39], s67 offen offset:0 sc1\n"
    "s_waitcnt lgkmcnt(2)\n"
    "v_pk_add_f32 v[50:51], v[50:51], v[60:61]\n"
    "v_pk_add_f32 v[52:53], v[52:53], v[62:63]\n"
    "s_waitcnt lgkmcnt(0)\n"
    "v_pk_add_f32 v[64:65], v[64:65], v[72:73]\n"
    "v_pk_add_f32 v[66:67], v[66:67], v[74:75]\n"
    "v_cvt_pk_bf16_f32 v198, v50, v51\n"
    "v_cvt_pk_bf16_f32 v199, v52, v53\n"
    "v_cvt_pk_bf16_f32 v200, v64, v65\n"
    "v_cvt_pk_bf16_f32 v201, v66, v67\n"
    "buffer_store_dwordx4 v[198:201], v192, s[36:39], s69 offen offset:0 sc1\n"
    "ds_read_b64_tr_b16 a[16:17], v20 offset:8192\n"
    "ds_read_b64_tr_b16 a[18:19], v20 offset:8704\n"
    "ds_read_b64_tr_b16 a[20:21], v21 offset:8192\n"
    "ds_read_b64_tr_b16 a[22:23], v21 offset:8704\n"
    "ds_read_b64_tr_b16 a[24:25], v22 offset:8192\n"
    "ds_read_b64_tr_b16 a[26:27], v22 offset:8704\n"
    "ds_read_b64_tr_b16 a[28:29], v23 offset:8192\n"
    "ds_read_b64_tr_b16 a[30:31], v23 offset:8704\n"
    "s_waitcnt vmcnt(16)\n"
    "s_nop 0\n"
    "ds_read_b64_tr_b16 a[32:33], v20 offset:12288\n"
    "ds_read_b64_tr_b16 a[34:35], v20 offset:12800\n"
    "ds_read_b64_tr_b16 a[36:37], v21 offset:12288\n"
    "ds_read_b64_tr_b16 a[38:39], v21 offset:12800\n"
    "ds_read_b64_tr_b16 a[40:41], v22 offset:12288\n"
    "ds_read_b64_tr_b16 a[42:43], v22 offset:12800\n"
    "ds_read_b64_tr_b16 a[44:45], v23 offset:12288\n"
    "ds_read_b64_tr_b16 a[46:47], v23 offset:12800\n"
    "s_waitcnt vmcnt(12)\n"
    "ds_read_b64_tr_b16 a[48:49], v20 offset:16384\n"
    "ds_read_b64_tr_b16 a[50:51], v20 offset:16896\n"
    "ds_read_b64_tr_b16 a[52:53], v21 offset:16384\n"
    "ds_read_b64_tr_b16 a[54:55], v21 offset:16896\n"
    "ds_read_b64_tr_b16 a[56:57], v22 offset:16384\n"
    "ds_read_b64_tr_b16 a[58:59], v22 offset:16896\n"
    "ds_read_b64_tr_b16 a[60:61], v23 offset:16384\n"
    "ds_read_b64_tr_b16 a[62:63], v23 offset:16896\n"
    "s_waitcnt vmcnt(8)\n"
    "ds_read_b64_tr_b16 a[64:65], v20 offset:20480\n"
    "ds_read_b64_tr_b16 a[66:67], v20 offset:20992\n"
    "ds_read_b64_tr_b16 a[68:69], v21 offset:20480\n"
    "ds_read_b64_tr_b16 a[70:71], v21 offset:20992\n"
    "ds_read_b64_tr_b16 a[72:73], v22 offset:20480\n"
    "ds_read_b64_tr_b16 a[74:75], v22 offset:20992\n"
    "ds_read_b64_tr_b16 a[76:77], v23 offset:20480\n"
    "ds_read_b64_tr_b16 a[78:79], v23 offset:20992\n"
    "s_add_u32 m0, s62, 0x1000\n"
    "s_add_u32 s57, s63, 0x118000\n"
    "s_add_u32 s72, s63, 0xfff58000\n"
    "s_min_u32 s57, s57, s72\n"
    "buffer_load_dwordx4 v12, s[40:43], s57 offen offset:0 lds\n"
    "s_waitcnt vmcnt(8)\n"
    "v_lshlrev_b32_e64 v30, 16, v26\n"
    "v_and_b32 v31, 0xffff0000, v26\n"
    "v_lshlrev_b32_e64 v32, 16, v27\n"
    "v_and_b32 v33, 0xffff0000, v27\n"
    "v_add_u32_e64 v29, s62, v3\n"
    "s_sub_u32 s55, s51, 1\n"
    "s_and_b32 s55, s55, 3\n"
    "s_mul_i32 s55, s55, 0xa000\n"
    "buffer_load_dwordx4 v13, s[40:43], s57 offen offset:1024 lds\n"
    "v_add_u32_e64 v34, s55, v3\n"
    "s_sub_u32 s55, s51, 2\n"
    "s_and_b32 s55, s55, 3\n"
    "s_mul_i32 s55, s55, 0xa000\n"
    "v_add_u32_e64 v35, s55, v3\n"
    "s_sub_u32 s55, s51, 3\n"
    "s_and_b32 s55, s55, 3\n"
    "s_mul_i32 s55, s55, 0xa000\n"
    "buffer_load_dwordx4 v14, s[40:43], s57 offen offset:2048 lds\n"
    "v_add_u32 v36, s55, v3\n"
    "s_lshl_b32 s55, s58, 18\n"
    "s_lshl_b32 s56, s51, 16\n"
    "s_add_u32 s55, s55, s56\n"
    "s_add_u32 s55, s55, 0x520000\n"
    "s_mul_i32 s84, s58, 0x38000\n"
    "s_mul_i32 s56, s51, 0xe000\n"
    "s_add_u32 s84, s84, s56\n"
    "buffer_load_dwordx4 v15, s[40:43], s57 offen offset:3072 lds\n"
    "s_add_u32 s84, s84, 0x720000\n"
    "v_add_u32 v37, s84, v7\n"
    "v_add_u32 v7, s55, v7\n"
    "s_lshl_b32 s84, s70, 13\n"
    "s_mov_b32 s4, s84\n"
    "s_add_u32 s5, s84, 0x2000\n"
    "s_add_u32 s55, s84, 0xffff2000\n"
    "s_min_u32 s5, s5, s55\n"
    "s_add_u32 m0, s62, 0x6000\n"
    "s_add_u32 s57, s63, 0x150000\n"
    "s_add_u32 s72, s63, 0xfff90000\n"
    "s_min_u32 s57, s57, s72\n"
    "buffer_load_dwordx4 v12, s[40:43], s57 offen offset:0 lds\n"
    "s_add_u32 s6, s84, 0x4000\n"
    "s_add_u32 s55, s84, 0xffff4000\n"
    "s_min_u32 s6, s6, s55\n"
    "s_add_u32 s7, s84, 0x6000\n"
    "s_add_u32 s55, s84, 0xffff6000\n"
    "s_min_u32 s7, s7, s55\n"
    "s_add_u32 s8, s84, 0x8000\n"
    "s_add_u32 s55, s84, 0xffff8000\n"
    "buffer_load_dwordx4 v13, s[40:43], s57 offen offset:1024 lds\n"
    "s_min_u32 s8, s8, s55\n"
    "s_add_u32 s9, s84, 0xa000\n"
    "s_add_u32 s55, s84, 0xffffa000\n"
    "s_min_u32 s9, s9, s55\n"
    "s_add_u32 s10, s84, 0xc000\n"
    "s_add_u32 s55, s84, 0xffffc000\n"
    "s_min_u32 s10, s10, s55\n"
    "s_add_u32 s11, s84, 0xe000\n"
    "buffer_load_dwordx4 v14, s[40:43], s57 offen offset:2048 lds\n"
    "s_add_u32 s55, s84, 0xffffe000\n"
    "s_min_u32 s11, s11, s55\n"
    "s_add_u32 s12, s84, 0x1000\n"
    "s_add_u32 s55, s84, 0xffff1000\n"
    "s_min_u32 s12, s12, s55\n"
    "s_add_u32 s13, s84, 0x3000\n"
    "s_add_u32 s55, s84, 0xffff3000\n"
    "s_min_u32 s13, s13, s55\n"
    "buffer_load_dwordx4 v15, s[40:43], s57 offen offset:3072 lds\n"
    "s_add_u32 s14, s84, 0x5000\n"
    "s_add_u32 s55, s84, 0xffff5000\n"
    "s_min_u32 s14, s14, s55\n"
    "s_add_u32 s15, s84, 0x7000\n"
    "s_add_u32 s55, s84, 0xffff7000\n"
    "s_min_u32 s15, s15, s55\n"
    "s_add_u32 s16, s84, 0x9000\n"
    "s_add_u32 s55, s84, 0xffff9000\n"
    "s_waitcnt lgkmcnt(15)\n"
    "s_add_u32 m0, s62, 0x2000\n"
    "s_add_u32 s57, s63, 0x188000\n"
    "s_add_u32 s72, s63, 0xfffc8000\n"
    "s_min_u32 s57, s57, s72\n"
    "buffer_load_dwordx4 v12, s[40:43], s57 offen offset:0 lds\n"
    "s_min_u32 s16, s16, s55\n"
    "s_add_u32 s17, s84, 0xb000\n"
    "s_add_u32 s55, s84, 0xffffb000\n"
    "s_min_u32 s17, s17, s55\n"
    "s_add_u32 s18, s84, 0xd000\n"
    "s_add_u32 s55, s84, 0xffffd000\n"
    "s_min_u32 s18, s18, s55\n"
    "s_add_u32 s19, s84, 0xf000\n"
    "buffer_load_dwordx4 v13, s[40:43], s57 offen offset:1024 lds\n"
    "s_add_u32 s55, s84, 0xfffff000\n"
    "s_min_u32 s19, s19, s55\n"
    "v_and_b32 v8, 1, v5\n"
    "v_lshlrev_b32 v8, 10, v8\n"
    "v_lshrrev_b32 v9, 1, v5\n"
    "v_lshl_add_u32 v8, v9, 8, v8\n"
    "v_lshl_add_u32 v38, v6, 4, v8\n"
    "v_lshlrev_b32_e64 v39, 3, v6\n"
    "buffer_load_dwordx4 v14, s[40:43], s57 offen offset:2048 lds\n"
    "s_lshl2_add_u32 s56, s60, s51\n"
    "s_lshl_b32 s55, s56, 7\n"
    "s_add_u32 s86, s55, 0x30000\n"
    "s_lshl_b32 s56, s56, 11\n"
    "s_lshl_b32 s55, s58, 18\n"
    "s_add_u32 s55, s55, s56\n"
    "s_add_u32 s85, s55, 0x520000\n"
    "v_and_b32 v8, 1, v5\n"
    "buffer_load_dwordx4 v15, s[40:43], s57 offen offset:3072 lds\n"
    "v_lshlrev_b32 v8, 5, v8\n"
    "v_lshrrev_b32 v9, 1, v5\n"
    "v_lshl_add_u32 v8, v9, 4, v8\n"
    "v_lshl_add_u32 v8, v6, 10, v8\n"
    "s_lshl2_add_u32 s55, s60, s51\n"
    "s_lshl_b32 s55, s55, 14\n"
    "s_lshl_b32 s56, s58, 7\n"
    "s_add_u32 s55, s55, s56\n"
    "s_add_u32 m0, s62, 0x3000\n"
    "s_add_u32 s57, s63, 0x54000\n"
    "s_add_u32 s72, s63, 0xffe94000\n"
    "s_min_u32 s57, s57, s72\n"
    "buffer_load_dwordx4 v12, s[40:43], s57 offen offset:0 lds\n"
    "v_add_u32_e64 v40, s55, v8\n"
    "s_or_b32 s55, s50, s51\n"
    "s_min_u32 s55, s55, 1\n"
    "s_lshl_b32 s55, s55, 30\n"
    "v_add_u32 v48, s55, v48\n"
    "v_readfirstlane_b32 s88, v68\n"
    "v_readfirstlane_b32 s89, v69\n"
    "s_movk_i32 s83, 0x3f80\n"
    "buffer_load_dwordx4 v13, s[40:43], s57 offen offset:1024 lds\n"
    "s_mov_b32 s82, 1.0\n"
    "s_lshl_b32 s55, s58, 12\n"
    "s_add_u32 s55, s55, 0x10000\n"
    "s_lshl2_add_u32 s56, s59, s51\n"
    "s_lshl_b32 s56, s56, 5\n"
    "s_add_u32 s87, s56, s55\n"
    "s_mul_i32 s56, s51, 0x380\n"
    "s_add_u32 s90, s56, s55\n"
    "buffer_load_dwordx4 v14, s[40:43], s57 offen offset:2048 lds\n"
    "v_min_u32_e64 v41, 27, v4\n"
    "v_lshlrev_b32 v41, 5, v41\n"
    "s_waitcnt vmcnt(15)\n"
    "buffer_load_dwordx4 v[42:45], v2, s[36:39], s76 offen offset:0 sc1\n"
    "buffer_load_dwordx4 v[50:53], v2, s[36:39], s77 offen offset:0 sc1\n"
    "s_barrier\n"
    "buffer_load_dwordx4 v15, s[40:43], s57 offen offset:3072 lds\n"
    "s_waitcnt lgkmcnt(8)\n"
    "s_add_u32 m0, s62, 0x4000\n"
    "s_add_u32 s57, s63, 0x8c000\n"
    "s_add_u32 s72, s63, 0xffecc000\n"
    "s_min_u32 s57, s57, s72\n"
    "buffer_load_dwordx4 v12, s[40:43], s57 offen offset:0 lds\n"
    "buffer_load_dwordx4 v13, s[40:43], s57 offen offset:1024 lds\n"
    "buffer_load_dwordx4 v14, s[40:43], s57 offen offset:2048 lds\n"
    "s_waitcnt vmcnt(17)\n"
    "ds_read_b64_tr_b16 a[80:81], v20 offset:4096\n"
    "ds_read_b64_tr_b16 a[82:83], v20 offset:4608\n"
    "ds_read_b64_tr_b16 a[84:85], v21 offset:4096\n"
    "ds_read_b64_tr_b16 a[86:87], v21 offset:4608\n"
    "ds_read_b64_tr_b16 a[88:89], v22 offset:4096\n"
    "ds_read_b64_tr_b16 a[90:91], v22 offset:4608\n"
    "ds_read_b64_tr_b16 a[92:93], v23 offset:4096\n"
    "ds_read_b64_tr_b16 a[94:95], v23 offset:4608\n"
    "s_waitcnt vmcnt(13)\n"
    "ds_read_b64_tr_b16 a[96:97], v20 offset:24576\n"
    "ds_read_b64_tr_b16 a[98:99], v20 offset:25088\n"
    "ds_read_b64_tr_b16 a[100:101], v21 offset:24576\n"
    "ds_read_b64_tr_b16 a[102:103], v21 offset:25088\n"
    "ds_read_b64_tr_b16 a[104:105], v22 offset:24576\n"
    "ds_read_b64_tr_b16 a[106:107], v22 offset:25088\n"
    "ds_read_b64_tr_b16 a[108:109], v23 offset:24576\n"
    "ds_read_b64_tr_b16 a[110:111], v23 offset:25088\n"
    "s_waitcnt vmcnt(9)\n"
    "ds_read_b64_tr_b16 a[112:113], v20 offset:8192\n"
    "ds_read_b64_tr_b16 a[114:115], v20 offset:8704\n"
    "ds_read_b64_tr_b16 a[116:117], v21 offset:8192\n"
    "ds_read_b64_tr_b16 a[118:119], v21 offset:8704\n"
    "ds_read_b64_tr_b16 a[120:121], v22 offset:8192\n"
    "ds_read_b64_tr_b16 a[122:123], v22 offset:8704\n"
    "ds_read_b64_tr_b16 a[124:125], v23 offset:8192\n"
    "ds_read_b64_tr_b16 a[126:127], v23 offset:8704\n"
    "buffer_load_dwordx4 v15, s[40:43], s57 offen offset:3072 lds\n"
    "s_waitcnt lgkmcnt(15)\n"
    "s_nop 0\n"
    "s_add_u32 m0, s62, 0x5000\n"
    "s_add_u32 s57, s63, 0xc4000\n"
    "s_add_u32 s72, s63, 0xfff04000\n"
    "s_min_u32 s57, s57, s72\n"
    "buffer_load_dwordx4 v12, s[40:43], s57 offen offset:0 lds\n"
    "s_waitcnt vmcnt(5)\n"
    "ds_read_b64_tr_b16 a[144:145], v20 offset:12288\n"
    "ds_read_b64_tr_b16 a[146:147], v20 offset:12800\n"
    "ds_read_b64_tr_b16 a[148:149], v21 offset:12288\n"
    "ds_read_b64_tr_b16 a[150:151], v21 offset:12800\n"
    "ds_read_b64_tr_b16 a[152:153], v22 offset:12288\n"
    "ds_read_b64_tr_b16 a[154:155], v22 offset:12800\n"
    "ds_read_b64_tr_b16 a[156:157], v23 offset:12288\n"
    "ds_read_b64_tr_b16 a[158:159], v23 offset:12800\n"
    "buffer_load_dwordx4 v13, s[40:43], s57 offen offset:1024 lds\n"
    "buffer_load_dwordx4 v14, s[40:43], s57 offen offset:2048 lds\n"
    "buffer_load_dwordx4 v15, s[40:43], s57 offen offset:3072 lds\n"
    "s_add_u32 m0, s62, 0x1000\n"
    "s_add_u32 s57, s63, 0xfc000\n"
    "s_add_u32 s72, s63, 0xfff3c000\n"
    "s_min_u32 s57, s57, s72\n"
    "buffer_load_dwordx4 v12, s[40:43], s57 offen offset:0 lds\n"
    "buffer_load_dwordx4 v13, s[40:43], s57 offen offset:1024 lds\n"
    "s_waitcnt vmcnt(6)\n"
    "ds_read_b64_tr_b16 a[160:161], v20 offset:16384\n"
    "ds_read_b64_tr_b16 a[162:163], v20 offset:16896\n"
    "ds_read_b64_tr_b16 a[164:165], v21 offset:16384\n"
    "ds_read_b64_tr_b16 a[166:167], v21 offset:16896\n"
    "ds_read_b64_tr_b16 a[168:169], v22 offset:16384\n"
    "ds_read_b64_tr_b16 a[170:171], v22 offset:16896\n"
    "ds_read_b64_tr_b16 a[172:173], v23 offset:16384\n"
    "ds_read_b64_tr_b16 a[174:175], v23 offset:16896\n"
    "buffer_load_dwordx4 v14, s[40:43], s57 offen offset:2048 lds\n"
    "v_max3_u32 v8, v43, v45, v51\n"
    "v_max_u32_e64 v8, v8, v53\n"
    "v_cmp_eq_u32 vcc, 0xffbfffbf, v8\n"
    "s_nop 0\n"
    "s_cbranch_vccnz .Lk_slow_4\n"
    ".Lk_back_5:\n"
    "; ---- LN2, HN published\n"
    "v_dot2_f32_bf16 v96, v42, s83, v96\n"
    "buffer_load_dwordx4 v15, s[40:43], s57 offen offset:3072 lds\n"
    "v_dot2_f32_bf16 v97, v42, s82, v97\n"
    "v_dot2_f32_bf16 v98, v43, s83, v98\n"
    "v_dot2_f32_bf16 v99, v43, s82, v99\n"
    "v_dot2_f32_bf16 v100, v44, s83, v100\n"
    "v_dot2_f32_bf16 v101, v44, s82, v101\n"
    "v_dot2_f32_bf16 v102, v45, s83, v102\n"
    "v_dot2_f32_bf16 v103, v45, s82, v103\n"
    "v_dot2_f32_bf16 v104, v50, s83, v104\n"
    "v_dot2_f32_bf16 v105, v50, s82, v105\n"
    "v_dot2_f32_bf16 v106, v51, s83, v106\n"
    "v_dot2_f32_bf16 v107, v51, s82, v107\n"
    "v_dot2_f32_bf16 v108, v52, s83, v108\n"
    "v_dot2_f32_bf16 v109, v52, s82, v109\n"
    "v_dot2_f32_bf16 v110, v53, s83, v110\n"
    "v_dot2_f32_bf16 v111, v53, s82, v111\n"
    "v_pk_add_f32 v[60:61], v[96:97], v[100:101]\n"
    "s_waitcnt lgkmcnt(15)\n"
    "s_add_u32 m0, s62, 0x6000\n"
    "s_add_u32 s57, s63, 0x134000\n"
    "s_add_u32 s72, s63, 0xfff74000\n"
    "s_min_u32 s57, s57, s72\n"
    "buffer_load_dwordx4 v12, s[40:43], s57 offen offset:0 lds\n"
    "v_pk_mul_f32 v[64:65], v[96:97], v[96:97]\n"
    "v_pk_add_f32 v[62:63], v[98:99], v[102:103]\n"
    "v_pk_mul_f32 v[66:67], v[98:99], v[98:99]\n"
    "v_pk_fma_f32 v[64:65], v[100:101], v[100:101], v[64:65]\n"
    "v_pk_fma_f32 v[66:67], v[102:103], v[102:103], v[66:67]\n"
    "v_pk_add_f32 v[60:61], v[60:61], v[104:105]\n"
    "v_pk_add_f32 v[62:63], v[62:63], v[106:107]\n"
    "v_pk_fma_f32 v[64:65], v[104:105], v[104:105], v[64:65]\n"
    "v_pk_fma_f32 v[66:67], v[106:107], v[106:107], v[66:67]\n"
    "v_pk_add_f32 v[60:61], v[60:61], v[108:109]\n"
    "v_pk_add_f32 v[62:63], v[62:63], v[110:111]\n"
    "v_pk_fma_f32 v[64:65], v[108:109], v[108:109], v[64:65]\n"
    "v_pk_fma_f32 v[66:67], v[110:111], v[110:111], v[66:67]\n"
    "v_permlane32_swap_b32 v60, v62\n"
    "v_permlane32_swap_b32 v61, v63\n"
    "buffer_load_dwordx4 v13, s[40:43], s57 offen offset:1024 lds\n"
    "v_permlane32_swap_b32 v64, v66\n"
    "v_pk_add_f32 v[60:61], v[60:61], v[62:63]\n"
    "v_permlane32_swap_b32 v65, v67\n"
    "v_pk_add_f32 v[64:65], v[64:65], v[66:67]\n"
    "v_permlane16_swap_b32 v60, v61\n"
    "v_add_f32 v62, v60, v61\n"
    "v_permlane16_swap_b32 v64, v65\n"
    "v_add_f32 v63, v64, v65\n"
    "v_add_f32_dpp v62, v62, v62 quad_perm:[2,3,0,1] row_mask:0xf bank_mask:0xf\n"
    "s_nop 0\n"
    "v_add_f32_dpp v63, v63, v63 quad_perm:[2,3,0,1] row_mask:0xf bank_mask:0xf\n"
    "v_add_f32_dpp v62, v62, v62 row_ror:4 row_mask:0xf bank_mask:0xf\n"
    "s_nop 0\n"
    "v_add_f32_dpp v63, v63, v63 row_ror:4 row_mask:0xf bank_mask:0xf\n"
    "buffer_load_dwordx4 v14, s[40:43], s57 offen offset:2048 lds\n"
    "v_add_f32_dpp v62, v62, v62 row_ror:8 row_mask:0xf bank_mask:0xf\n"
    "v_add_f32_dpp v63, v63, v63 row_ror:8 row_mask:0xf bank_mask:0xf\n"
    "s_mov_b64 exec, s[74:75]\n"
    "ds_write_b64 v54, v[62:63] offset:256\n"
    "s_mov_b64 exec, -1\n"
    "s_waitcnt lgkmcnt(0)\n"
    "s_barrier\n"
    "ds_read_b128 v[60:63], v49 offset:256\n"
    "ds_read_b128 v[64:67], v49 offset:272\n"
    "s_waitcnt lgkmcnt(1)\n"
    "v_pk_add_f32 v[60:61], v[60:61], v[62:63]\n"
    "s_waitcnt lgkmcnt(0)\n"
    "v_pk_add_f32 v[64:65], v[64:65], v[66:67]\n"
    "v_pk_add_f32 v[64:65], v[64:65], v[60:61]\n"
    "v_mul_f32 v26, 0x3b000000, v64\n"
    "v_mul_f32 v65, 0x3b000000, v65\n"
    "v_fma_f32 v65, -v26, v26, v65\n"
    "buffer_load_dwordx4 v15, s[40:43], s57 offen offset:3072 lds\n"
    "v_max_f32_e64 v65, 0, v65\n"
    "v_add_f32 v66, 0x3727c5ac, v65\n"
    "v_fmamk_f32 v66, v66, 0x3727c5ac, v65\n"
    "v_sqrt_f32 v27, v66\n"
    "v_rsq_f32 v60, v66\n"
    "s_nop 0\n"
    "v_mul_f32_e64 v64, -v26, v60\n"
    "v_mov_b32 v61, v60\n"
    "v_mov_b32 v65, v64\n"
    "s_nop 0\n"
    "v_permlane16_swap_b32 v60, v61\n"
    "v_permlane16_swap_b32 v64, v65\n"
    "v_pk_mov_b32 v[62:63], v[60:61], v[60:61] op_sel:[0,1]\n"
    "v_pk_mov_b32 v[66:67], v[64:65], v[64:65] op_sel:[0,1]\n"
    "s_add_u32 m0, s62, 0x2000\n"
    "s_add_u32 s57, s63, 0x16c000\n"
    "s_add_u32 s72, s63, 0xfffac000\n"
    "s_min_u32 s57, s57, s72\n"
    "buffer_load_dwordx4 v12, s[40:43], s57 offen offset:0 lds\n"
    "v_permlane32_swap_b32 v60, v62\n"
    "v_permlane32_swap_b32 v61, v63\n"
    "v_permlane32_swap_b32 v64, v66\n"
    "v_permlane32_swap_b32 v65, v67\n"
    "s_mov_b32 vcc_lo, 0x55555555\n"
    "s_mov_b32 vcc_hi, vcc_lo\n"
    "v_pk_fma_f32 v[72:73], v[96:97], v[60:61], v[64:65]\n"
    "v_pk_fma_f32 v[74:75], v[98:99], v[62:63], v[66:67]\n"
    "v_pk_fma_f32 v[76:77], v[100:101], v[60:61], v[64:65]\n"
    "v_pk_fma_f32 v[78:79], v[102:103], v[62:63], v[66:67]\n"
    "v_cvt_pk_bf16_f32 v44, v76, v77\n"
    "v_cvt_pk_bf16_f32 v45, v78, v79\n"
    "v_cvt_pk_bf16_f32 v42, v72, v73\n"
    "v_cvt_pk_bf16_f32 v43, v74, v75\n"
    "s_nop 0\n"
    "v_cndmask_b32_dpp v72, v44, v42, vcc quad_perm:[1,0,3,2] row_mask:0xf bank_mask:0xf\n"
    "v_cndmask_b32_dpp v73, v45, v43, vcc quad_perm:[1,0,3,2] row_mask:0xf bank_mask:0xf\n"
    "s_not_b64 vcc, vcc\n"
    "v_cndmask_b32_dpp v74, v42, v44, vcc quad_perm:[1,0,3,2] row_mask:0xf bank_mask:0xf\n"
    "v_cndmask_b32_dpp v75, v43, v45, vcc quad_perm:[1,0,3,2] row_mask:0xf bank_mask:0xf\n"
    "buffer_store_dwordx4 v[72:75], v3, s[36:39], s78 offen offset:0 sc1\n"
    "v_pk_fma_f32 v[80:81], v[104:105], v[60:61], v[64:65]\n"
    "v_pk_fma_f32 v[82:83], v[106:107], v[62:63], v[66:67]\n"
    "v_pk_fma_f32 v[84:85], v[108:109], v[60:61], v[64:65]\n"
    "v_pk_fma_f32 v[86:87], v[110:111], v[62:63], v[66:67]\n"
    "v_cvt_pk_bf16_f32 v50, v80, v81\n"
    "v_cvt_pk_bf16_f32 v51, v82, v83\n"
    "v_cvt_pk_bf16_f32 v52, v84, v85\n"
    "v_cvt_pk_bf16_f32 v53, v86, v87\n"
    "s_nop 0\n"
    "v_cndmask_b32_dpp v82, v50, v52, vcc quad_perm:[1,0,3,2] row_mask:0xf bank_mask:0xf\n"
    "v_cndmask_b32_dpp v83, v51, v53, vcc quad_perm:[1,0,3,2] row_mask:0xf bank_mask:0xf\n"
    "s_not_b64 vcc, vcc\n"
    "v_cndmask_b32_dpp v80, v52, v50, vcc quad_perm:[1,0,3,2] row_mask:0xf bank_mask:0xf\n"
    "v_cndmask_b32_dpp v81, v53, v51, vcc quad_perm:[1,0,3,2] row_mask:0xf bank_mask:0xf\n"
    "buffer_store_dwordx4 v[80:83], v3, s[36:39], s79 offen offset:0 sc1\n"
    "buffer_store_dwordx2 v[26:27], v58, s[36:39], s81 offen offset:0 sc1\n"
    "buffer_store_dwordx4 v[68:71], v48, s[36:39], 0 offen offset:0 sc1\n"
    "; ---- weights streamed\n"
    "s_barrier\n"
    "s_waitcnt vmcnt(13)\n"
    "ds_read_b64_tr_b16 a[176:177], v20 offset:20480\n"
    "ds_read_b64_tr_b16 a[178:179], v20 offset:20992\n"
    "ds_read_b64_tr_b16 a[180:181], v21 offset:20480\n"
    "ds_read_b64_tr_b16 a[182:183], v21 offset:20992\n"
    "ds_read_b64_tr_b16 a[184:185], v22 offset:20480\n"
    "ds_read_b64_tr_b16 a[186:187], v22 offset:20992\n"
    "ds_read_b64_tr_b16 a[188:189], v23 offset:20480\n"
    "ds_read_b64_tr_b16 a[190:191], v23 offset:20992\n"
    "s_waitcnt vmcnt(9)\n"
    "s_nop 0\n"
    "ds_read_b64_tr_b16 a[192:193], v20 offset:4096\n"
    "ds_read_b64_tr_b16 a[194:195], v20 offset:4608\n"
    "ds_read_b64_tr_b16 a[196:197], v21 offset:4096\n"
    "ds_read_b64_tr_b16 a[198:199], v21 offset:4608\n"
    "ds_read_b64_tr_b16 a[200:201], v22 offset:4096\n"
    "ds_read_b64_tr_b16 a[202:203], v22 offset:4608\n"
    "ds_read_b64_tr_b16 a[204:205], v23 offset:4096\n"
    "ds_read_b64_tr_b16 a[206:207], v23 offset:4608\n"
    "buffer_load_dwordx4 v13, s[40:43], s57 offen offset:1024 lds\n"
    "buffer_load_dwordx4 v14, s[40:43], s57 offen offset:2048 lds\n"
    "s_waitcnt vmcnt(7)\n"
    "s_nop 0\n"
    "ds_read_b64_tr_b16 a[208:209], v20 offset:24576\n"
    "ds_read_b64_tr_b16 a[210:211], v20 offset:25088\n"
    "ds_read_b64_tr_b16 a[212:213], v21 offset:24576\n"
    "ds_read_b64_tr_b16 a[214:215], v21 offset:25088\n"
    "ds_read_b64_tr_b16 a[216:217], v22 offset:24576\n"
    "ds_read_b64_tr_b16 a[218:219], v22 offset:25088\n"
    "ds_read_b64_tr_b16 a[220:221], v23 offset:24576\n"
    "ds_read_b64_tr_b16 a[222:223], v23 offset:25088\n"
    "buffer_load_dwordx4 v15, s[40:43], s57 offen offset:3072 lds\n"
    "s_add_u32 m0, s62, 0x3000\n"
    "s_add_u32 s57, s63, 0x1a4000\n"
    "s_add_u32 s72, s63, 0xfffe4000\n"
    "s_min_u32 s57, s57, s72\n"
    "s_nop 0\n"
    "buffer_load_dwordx4 v12, s[40:43], s57 offen offset:0 lds\n"
    "buffer_load_dwordx4 v13, s[40:43], s57 offen offset:1024 lds\n"
    "buffer_load_dwordx4 v14, s[40:43], s57 offen offset:2048 lds\n"
    "buffer_load_dwordx4 v15, s[40:43], s57 offen offset:3072 lds\n"
    "s_add_u32 m0, s62, 0x4000\n"
    "s_add_u32 s71, s64, 0x40000\n"
    "s_add_u32 s72, s64, 0xffe80000\n"
    "s_min_u32 s71, s71, s72\n"
    "buffer_load_dwordx4 v16, s[44:47], s71 offen offset:0 lds\n"
    "buffer_load_dwordx4 v17, s[44:47], s71 offen offset:1024 lds\n"
    "buffer_load_dwordx4 v18, s[44:47], s71 offen offset:2048 lds\n"
    "s_waitcnt vmcnt(7)\n"
    "ds_read_b64_tr_b16 a[224:225], v20 offset:8192\n"
    "ds_read_b64_tr_b16 a[226:227], v20 offset:8704\n"
    "ds_read_b64_tr_b16 a[228:229], v21 offset:8192\n"
    "ds_read_b64_tr_b16 a[230:231], v21 offset:8704\n"
    "ds_read_b64_tr_b16 a[232:233], v22 offset:8192\n"
    "ds_read_b64_tr_b16 a[234:235], v22 offset:8704\n"
    "ds_read_b64_tr_b16 a[236:237], v23 offset:8192\n"
    "ds_read_b64_tr_b16 a[238:239], v23 offset:8704\n"
    "buffer_load_dwordx4 v19, s[44:47], s71 offen offset:3072 lds\n"
    "s_waitcnt lgkmcnt(15)\n"
    "s_nop 0\n"
    "s_add_u32 m0, s62, 0x5000\n"
    "s_add_u32 s71, s64, 0x80000\n"
    "s_add_u32 s72, s64, 0xffec0000\n"
    "s_min_u32 s71, s71, s72\n"
    "buffer_load_dwordx4 v16, s[44:47], s71 offen offset:0 lds\n"
    "buffer_load_dwordx4 v17, s[44:47], s71 offen offset:1024 lds\n"
    "s_waitcnt vmcnt(6)\n"
    "ds_read_b64_tr_b16 a[240:241], v20 offset:12288\n"
    "ds_read_b64_tr_b16 a[242:243], v20 offset:12800\n"
    "ds_read_b64_tr_b16 a[244:245], v21 offset:12288\n"
    "ds_read_b64_tr_b16 a[246:247], v21 offset:12800\n"
    "ds_read_b64_tr_b16 a[248:249], v22 offset:12288\n"
    "ds_read_b64_tr_b16 a[250:251], v22 offset:12800\n"
    "ds_read_b64_tr_b16 a[252:253], v23 offset:12288\n"
    "ds_read_b64_tr_b16 a[254:255], v23 offset:12800\n"
    "buffer_load_dwordx4 v18, s[44:47], s71 offen offset:2048 lds\n"
    "buffer_load_dwordx4 v19, s[44:47], s71 offen offset:3072 lds\n"
    "s_add_u32 m0, s62, 0x1000\n"
    "s_add_u32 s71, s64, 0xc0000\n"
    "s_add_u32 s72, s64, 0xfff00000\n"
    "s_min_u32 s71, s71, s72\n"
    "s_nop 0\n"
    "buffer_load_dwordx4 v16, s[44:47], s71 offen offset:0 lds\n"
    "buffer_load_dwordx4 v17, s[44:47], s71 offen offset:1024 lds\n"
    "buffer_load_dwordx4 v18, s[44:47], s71 offen offset:2048 lds\n"
    "buffer_load_dwordx4 v19, s[44:47], s71 offen offset:3072 lds\n"
    "s_waitcnt lgkmcnt(15)\n"
    "s_add_u32 m0, s62, 0x6000\n"
    "s_add_u32 s71, s64, 0x100000\n"
    "s_add_u32 s72, s64, 0xfff40000\n"
    "s_min_u32 s71, s71, s72\n"
    "buffer_load_dwordx4 v16, s[44:47], s71 offen offset:0 lds\n"
    "buffer_load_dwordx4 v17, s[44:47], s71 offen offset:1024 lds\n"
    "buffer_load_dwordx4 v18, s[44:47], s71 offen offset:2048 lds\n"
    "buffer_load_dwordx4 v19, s[44:47], s71 offen offset:3072 lds\n"
    "s_cmp_lg_u32 s61, 0\n"
    "s_cbranch_scc1 .Lk_idle\n"
    "; ---- first GEMM\n"
    "buffer_load_dwordx4 v[102:105], v7, s[36:39], s4 offen offset:0 sc1\n"
    "s_nop 5\n"
    "buffer_load_dwordx4 v[106:109], v7, s[36:39], s4 offen offset:256 sc1\n"
    "s_nop 5\n"
    "buffer_load_dwordx4 v[110:113], v7, s[36:39], s5 offen offset:0 sc1\n"
    "s_nop 5\n"
    "buffer_load_dwordx4 v[114:117], v7, s[36:39], s5 offen offset:256 sc1\n"
    "s_nop 5\n"
    "buffer_load_dwordx4 v[118:121], v7, s[36:39], s6 offen offset:0 sc1\n"
    "s_nop 5\n"
    "buffer_load_dwordx4 v[122:125], v7, s[36:39], s6 offen offset:256 sc1\n"
    "s_nop 5\n"
    "buffer_load_dwordx4 v[126:129], v7, s[36:39], s7 offen offset:0 sc1\n"
    "s_nop 5\n"
    "buffer_load_dwordx4 v[130:133], v7, s[36:39], s7 offen offset:256 sc1\n"
    "s_nop 5\n"
    "buffer_load_dwordx4 v[134:137], v7, s[36:39], s8 offen offset:0 sc1\n"
    "s_nop 5\n"
    "buffer_load_dwordx4 v[138:141], v7, s[36:39], s8 offen offset:256 sc1\n"
    "s_nop 5\n"
    "buffer_load_dwordx4 v[142:145], v7, s[36:39], s9 offen offset:0 sc1\n"
    "s_nop 5\n"
    "buffer_load_dwordx4 v[146:149], v7, s[36:39], s9 offen offset:256 sc1\n"
    "s_nop 5\n"
    "s_mov_b32 s74, 0xbdd2d3e8\n"
    "v_mov_b32 v240, 0xc0135761\n"
    "s_waitcnt vmcnt(10)\n"
    "v_max3_u32 v28, v103, v105, v107\n"
    "s_waitcnt vmcnt(9)\n"
    "v_max3_u32 v28, v28, v109, v111\n"
    "s_waitcnt vmcnt(8)\n"
    "v_max3_u32 v28, v28, v113, v115\n"
    "v_max_u32 v28, v28, v117\n"
    "v_cmp_eq_u32 vcc, 0xffbfffbf, v28\n"
    "s_nop 0\n"
    "s_cbranch_vccnz .Lk_slow_7\n"
    ".Lk_back_8:\n"
    "v_mfma_f32_16x16x32_bf16 v[54:57], a[4:7], v[102:105], 0\n"
    "buffer_load_dwordx4 v[150:153], v7, s[36:39], s10 offen offset:0 sc1\n"
    "v_mfma_f32_16x16x32_bf16 v[58:61], a[4:7], v[106:109], 0\n"
    "v_mfma_f32_16x16x32_bf16 v[70:73], a[8:11], v[102:105], 0\n"
    "buffer_load_dwordx4 v[154:157], v7, s[36:39], s10 offen offset:256 sc1\n"
    "v_mfma_f32_16x16x32_bf16 v[74:77], a[8:11], v[106:109], 0\n"
    "v_mfma_f32_16x16x32_bf16 v[86:89], a[12:15], v[102:105], 0\n"
    "buffer_load_dwordx4 v[158:161], v7, s[36:39], s11 offen offset:0 sc1\n"
    "v_mfma_f32_16x16x32_bf16 v[90:93], a[12:15], v[106:109], 0\n"
    "v_mfma_f32_16x16x32_bf16 v[12:15], a[0:3], v[102:105], v[30:33]\n"
    "buffer_load_dwordx4 v[162:165], v7, s[36:39], s11 offen offset:256 sc1\n"
    "v_mfma_f32_16x16x32_bf16 v[42:45], a[0:3], v[106:109], v[30:33]\n"
    "s_waitcnt vmcnt(8)\n"
    "v_max3_u32 v28, v119, v121, v123\n"
    "v_mfma_f32_16x16x32_bf16 v[54:57], a[20:23], v[110:113], v[54:57]\n"
    "buffer_load_dwordx4 v[166:169], v7, s[36:39], s12 offen offset:0 sc1\n"
    "v_max3_u32 v28, v28, v125, v127\n"
    "v_mfma_f32_16x16x32_bf16 v[58:61], a[20:23], v[114:117], v[58:61]\n"
    "v_max3_u32 v28, v28, v129, v131\n"
    "v_mfma_f32_16x16x32_bf16 v[70:73], a[24:27], v[110:113], v[70:73]\n"
    "buffer_load_dwordx4 v[170:173], v7, s[36:39], s12 offen offset:256 sc1\n"
    "v_max_u32 v28, v28, v133\n"
    "v_mfma_f32_16x16x32_bf16 v[74:77], a[24:27], v[114:117], v[74:77]\n"
    "v_cmp_eq_u32 vcc, 0xffbfffbf, v28\n"
    "v_mfma_f32_16x16x32_bf16 v[86:89], a[28:31], v[110:113], v[86:89]\n"
    "buffer_load_dwordx4 v[174:177], v7, s[36:39], s13 offen offset:0 sc1\n"
    "v_mfma_f32_16x16x32_bf16 v[90:93], a[28:31], v[114:117], v[90:93]\n"
    "s_cbranch_vccnz .Lk_slow_10\n"
    ".Lk_back_11:\n"
    "v_mfma_f32_16x16x32_bf16 v[12:15], a[16:19], v[110:113], v[12:15]\n"
    "buffer_load_dwordx4 v[178:181], v7, s[36:39], s13 offen offset:256 sc1\n"
    "v_mfma_f32_16x16x32_bf16 v[42:45], a[16:19], v[114:117], v[42:45]\n"
    "v_mfma_f32_16x16x32_bf16 v[54:57], a[36:39], v[118:121], v[54:57]\n"
    "buffer_load_dwordx4 v[182:185], v7, s[36:39], s14 offen offset:0 sc1\n"
    "v_mfma_f32_16x16x32_bf16 v[58:61], a[36:39], v[122:125], v[58:61]\n"
    "v_mfma_f32_16x16x32_bf16 v[70:73], a[40:43], v[118:121], v[70:73]\n"
    "buffer_load_dwordx4 v[186:189], v7, s[36:39], s14 offen offset:256 sc1\n"
    "v_mfma_f32_16x16x32_bf16 v[74:77], a[40:43], v[122:125], v[74:77]\n"
    "v_mfma_f32_16x16x32_bf16 v[86:89], a[44:47], v[118:121], v[86:89]\n"
    "buffer_load_dwordx4 v[190:193], v7, s[36:39], s15 offen offset:0 sc1\n"
    "v_mfma_f32_16x16x32_bf16 v[90:93], a[44:47], v[122:125], v[90:93]\n"
    "v_mfma_f32_16x16x32_bf16 v[12:15], a[32:35], v[118:121], v[12:15]\n"
    "buffer_load_dwordx4 v[194:197], v7, s[36:39], s15 offen offset:256 sc1\n"
    "v_mfma_f32_16x16x32_bf16 v[42:45], a[32:35], v[122:125], v[42:45]\n"
    "s_waitcnt vmcnt(12)\n"
    "v_max3_u32 v28, v135, v137, v139\n"
    "v_mfma_f32_16x16x32_bf16 v[54:57], a[52:55], v[126:129], v[54:57]\n"
    "buffer_load_dwordx4 v[198:201], v7, s[36:39], s16 offen offset:0 sc1\n"
    "v_max3_u32 v28, v28, v141, v143\n"
    "v_mfma_f32_16x16x32_bf16 v[58:61], a[52:55], v[130:133], v[58:61]\n"
    "v_max3_u32 v28, v28, v145, v147\n"
    "v_mfma_f32_16x16x32_bf16 v[70:73], a[56:59], v[126:129], v[70:73]\n"
    "buffer_load_dwordx4 v[202:205], v7, s[36:39], s16 offen offset:256 sc1\n"
    "v_max_u32 v28, v28, v149\n"
    "v_mfma_f32_16x16x32_bf16 v[74:77], a[56:59], v[130:133], v[74:77]\n"
    "v_cmp_eq_u32 vcc, 0xffbfffbf, v28\n"
    "v_mfma_f32_16x16x32_bf16 v[86:89], a[60:63], v[126:129], v[86:89]\n"
    "buffer_load_dwordx4 v[206:209], v7, s[36:39], s17 offen offset:0 sc1\n"
    "v_mfma_f32_16x16x32_bf16 v[90:93], a[60:63], v[130:133], v[90:93]\n"
    "s_cbranch_vccnz .Lk_slow_13\n"
    ".Lk_back_14:\n"
    "v_mfma_f32_16x16x32_bf16 v[12:15], a[48:51], v[126:129], v[12:15]\n"
    "buffer_load_dwordx4 v[210:213], v7, s[36:39], s17 offen offset:256 sc1\n"
    "v_mfma_f32_16x16x32_bf16 v[42:45], a[48:51], v[130:133], v[42:45]\n"
    "v_mfma_f32_16x16x32_bf16 v[54:57], a[68:71], v[134:137], v[54:57]\n"
    "buffer_load_dwordx4 v[214:217], v7, s[36:39], s18 offen offset:0 sc1\n"
    "v_mfma_f32_16x16x32_bf16 v[58:61], a[68:71], v[138:141], v[58:61]\n"
    "v_mfma_f32_16x16x32_bf16 v[70:73], a[72:75], v[134:137], v[70:73]\n"
    "v_mfma_f32_16x16x32_bf16 v[74:77], a[72:75], v[138:141], v[74:77]\n"
    "v_mfma_f32_16x16x32_bf16 v[86:89], a[76:79], v[134:137], v[86:89]\n"
    "buffer_load_dwordx4 v[218:221], v7, s[36:39], s18 offen offset:256 sc1\n"
    "v_mfma_f32_16x16x32_bf16 v[90:93], a[76:79], v[138:141], v[90:93]\n"
    "v_mfma_f32_16x16x32_bf16 v[12:15], a[64:67], v[134:137], v[12:15]\n"
    "v_mfma_f32_16x16x32_bf16 v[42:45], a[64:67], v[138:141], v[42:45]\n"
    "s_waitcnt vmcnt(14)\n"
    "v_max3_u32 v28, v151, v153, v155\n"
    "v_mfma_f32_16x16x32_bf16 v[54:57], a[84:87], v[142:145], v[54:57]\n"
    "buffer_load_dwordx4 v[222:225], v7, s[36:39], s19 offen offset:0 sc1\n"
    "v_max3_u32 v28, v28, v157, v159\n"
    "v_mfma_f32_16x16x32_bf16 v[58:61], a[84:87], v[146:149], v[58:61]\n"
    "v_max3_u32 v28, v28, v161, v163\n"
    "v_mfma_f32_16x16x32_bf16 v[70:73], a[88:91], v[142:145], v[70:73]\n"
    "v_max_u32 v28, v28, v165\n"
    "v_mfma_f32_16x16x32_bf16 v[74:77], a[88:91], v[146:149], v[74:77]\n"
    "v_cmp_eq_u32 vcc, 0xffbfffbf, v28\n"
    "v_mfma_f32_16x16x32_bf16 v[86:89], a[92:95], v[142:145], v[86:89]\n"
    "buffer_load_dwordx4 v[226:229], v7, s[36:39], s19 offen offset:256 sc1\n"
    "v_mfma_f32_16x16x32_bf16 v[90:93], a[92:95], v[146:149], v[90:93]\n"
    "s_cbranch_vccnz .Lk_slow_16\n"
    ".Lk_back_17:\n"
    "v_mfma_f32_16x16x32_bf16 v[12:15], a[80:83], v[142:145], v[12:15]\n"
    "v_mfma_f32_16x16x32_bf16 v[42:45], a[80:83], v[146:149], v[42:45]\n"
    "v_mfma_f32_16x16x32_bf16 v[54:57], a[100:103], v[150:153], v[54:57]\n"
    "buffer_load_dwordx4 v[102:105], v7, s[36:39], s4 offen offset:512 sc1\n"
    "v_mfma_f32_16x16x32_bf16 v[58:61], a[100:103], v[154:157], v[58:61]\n"
    "v_mfma_f32_16x16x32_bf16 v[70:73], a[104:107], v[150:153], v[70:73]\n"
    "v_mfma_f32_16x16x32_bf16 v[74:77], a[104:107], v[154:157], v[74:77]\n"
    "v_mfma_f32_16x16x32_bf16 v[86:89], a[108:111], v[150:153], v[86:89]\n"
    "buffer_load_dwordx4 v[106:109], v7, s[36:39], s4 offen offset:768 sc1\n"
    "v_mfma_f32_16x16x32_bf16 v[90:93], a[108:111], v[154:157], v[90:93]\n"
    "v_mfma_f32_16x16x32_bf16 v[12:15], a[96:99], v[150:153], v[12:15]\n"
    "v_mfma_f32_16x16x32_bf16 v[42:45], a[96:99], v[154:157], v[42:45]\n"
    "s_waitcnt vmcnt(14)\n"
    "v_max3_u32 v28, v167, v169, v171\n"
    "v_mfma_f32_16x16x32_bf16 v[54:57], a[116:119], v[158:161], v[54:57]\n"
    "buffer_load_dwordx4 v[110:113], v7, s[36:39], s5 offen offset:512 sc1\n"
    "v_max3_u32 v28, v28, v173, v175\n"
    "v_mfma_f32_16x16x32_bf16 v[58:61], a[116:119], v[162:165], v[58:61]\n"
    "v_max3_u32 v28, v28, v177, v179\n"
    "v_mfma_f32_16x16x32_bf16 v[70:73], a[120:123], v[158:161], v[70:73]\n"
    "v_max_u32 v28, v28, v181\n"
    "v_mfma_f32_16x16x32_bf16 v[74:77], a[120:123], v[162:165], v[74:77]\n"
    "v_cmp_eq_u32 vcc, 0xffbfffbf, v28\n"
    "v_mfma_f32_16x16x32_bf16 v[86:89], a[124:127], v[158:161], v[86:89]\n"
    "buffer_load_dwordx4 v[114:117], v7, s[36:39], s5 offen offset:768 sc1\n"
    "v_mfma_f32_16x16x32_bf16 v[90:93], a[124:127], v[162:165], v[90:93]\n"
    "s_cbranch_vccnz .Lk_slow_19\n"
    ".Lk_back_20:\n"
    "v_mfma_f32_16x16x32_bf16 v[12:15], a[112:115], v[158:161], v[12:15]\n"
    "v_mfma_f32_16x16x32_bf16 v[42:45], a[112:115], v[162:165], v[42:45]\n"
    "v_mfma_f32_16x16x32_bf16 v[54:57], a[132:135], v[166:169], v[54:57]\n"
    "buffer_load_dwordx4 v[118:121], v7, s[36:39], s6 offen offset:512 sc1\n"
    "v_mfma_f32_16x16x32_bf16 v[58:61], a[132:135], v[170:173], v[58:61]\n"
    "v_mfma_f32_16x16x32_bf16 v[70:73], a[136:139], v[166:169], v[70:73]\n"
    "v_mfma_f32_16x16x32_bf16 v[74:77], a[136:139], v[170:173], v[74:77]\n"
    "v_mfma_f32_16x16x32_bf16 v[86:89], a[140:143], v[166:169], v[86:89]\n"
    "buffer_load_dwordx4 v[122:125], v7, s[36:39], s6 offen offset:768 sc1\n"
    "v_mfma_f32_16x16x32_bf16 v[90:93], a[140:143], v[170:173], v[90:93]\n"
    "v_mfma_f32_16x16x32_bf16 v[12:15], a[128:131], v[166:169], v[12:15]\n"
    "v_mfma_f32_16x16x32_bf16 v[42:45], a[128:131], v[170:173], v[42:45]\n"
    "s_waitcnt vmcnt(14)\n"
    "v_max3_u32 v28, v183, v185, v187\n"
    "v_mfma_f32_16x16x32_bf16 v[54:57], a[148:151], v[174:177], v[54:57]\n"
    "buffer_load_dwordx4 v[126:129], v7, s[36:39], s7 offen offset:512 sc1\n"
    "v_max3_u32 v28, v28, v189, v191\n"
    "v_mfma_f32_16x16x32_bf16 v[58:61], a[148:151], v[178:181], v[58:61]\n"
    "v_max3_u32 v28, v28, v193, v195\n"
    "v_mfma_f32_16x16x32_bf16 v[70:73], a[152:155], v[174:177], v[70:73]\n"
    "v_max_u32 v28, v28, v197\n"
    "v_mfma_f32_16x16x32_bf16 v[74:77], a[152:155], v[178:181], v[74:77]\n"
    "v_cmp_eq_u32 vcc, 0xffbfffbf, v28\n"
    "v_mfma_f32_16x16x32_bf16 v[86:89], a[156:159], v[174:177], v[86:89]\n"
    "buffer_load_dwordx4 v[130:133], v7, s[36:39], s7 offen offset:768 sc1\n"
    "v_mfma_f32_16x16x32_bf16 v[90:93], a[156:159], v[178:181], v[90:93]\n"
    "s_cbranch_vccnz .Lk_slow_22\n"
    ".Lk_back_23:\n"
    "v_mfma_f32_16x16x32_bf16 v[12:15], a[144:147], v[174:177], v[12:15]\n"
    "v_mfma_f32_16x16x32_bf16 v[42:45], a[144:147], v[178:181], v[42:45]\n"
    "v_mfma_f32_16x16x32_bf16 v[54:57], a[164:167], v[182:185], v[54:57]\n"
    "buffer_load_dwordx4 v[134:137], v7, s[36:39], s8 offen offset:512 sc1\n"
    "v_mfma_f32_16x16x32_bf16 v[58:61], a[164:167], v[186:189], v[58:61]\n"
    "v_mfma_f32_16x16x32_bf16 v[70:73], a[168:171], v[182:185], v[70:73]\n"
    "v_mfma_f32_16x16x32_bf16 v[74:77], a[168:171], v[186:189], v[74:77]\n"
    "v_mfma_f32_16x16x32_bf16 v[86:89], a[172:175], v[182:185], v[86:89]\n"
    "buffer_load_dwordx4 v[138:141], v7, s[36:39], s8 offen offset:768 sc1\n"
    "v_mfma_f32_16x16x32_bf16 v[90:93], a[172:175], v[186:189], v[90:93]\n"
    "v_mfma_f32_16x16x32_bf16 v[12:15], a[160:163], v[182:185], v[12:15]\n"
    "v_mfma_f32_16x16x32_bf16 v[42:45], a[160:163], v[186:189], v[42:45]\n"
    "s_waitcnt vmcnt(14)\n"
    "v_max3_u32 v28, v199, v201, v203\n"
    "v_mfma_f32_16x16x32_bf16 v[54:57], a[180:183], v[190:193], v[54:57]\n"
    "buffer_load_dwordx4 v[142:145], v7, s[36:39], s9 offen offset:512 sc1\n"
    "v_max3_u32 v28, v28, v205, v207\n"
    "v_mfma_f32_16x16x32_bf16 v[58:61], a[180:183], v[194:197], v[58:61]\n"
    "v_max3_u32 v28, v28, v209, v211\n"
    "v_mfma_f32_16x16x32_bf16 v[70:73], a[184:187], v[190:193], v[70:73]\n"
    "v_max_u32_e64 v28, v28, v213\n"
    "v_mfma_f32_16x16x32_bf16 v[74:77], a[184:187], v[194:197], v[74:77]\n"
    "v_cmp_eq_u32 vcc, 0xffbfffbf, v28\n"
    "v_mfma_f32_16x16x32_bf16 v[86:89], a[188:191], v[190:193], v[86:89]\n"
    "buffer_load_dwordx4 v[146:149], v7, s[36:39], s9 offen offset:768 sc1\n"
    "v_mfma_f32_16x16x32_bf16 v[90:93], a[188:191], v[194:197], v[90:93]\n"
    "s_cbranch_vccnz .Lk_slow_25\n"
    ".Lk_back_26:\n"
    "v_mfma_f32_16x16x32_bf16 v[12:15], a[176:179], v[190:193], v[12:15]\n"
    "v_mfma_f32_16x16x32_bf16 v[42:45], a[176:179], v[194:197], v[42:45]\n"
    "v_mfma_f32_16x16x32_bf16 v[54:57], a[196:199], v[198:201], v[54:57]\n"
    "buffer_load_dwordx4 v[150:153], v7, s[36:39], s10 offen offset:512 sc1\n"
    "v_mfma_f32_16x16x32_bf16 v[58:61], a[196:199], v[202:205], v[58:61]\n"
    "v_mfma_f32_16x16x32_bf16 v[70:73], a[200:203], v[198:201], v[70:73]\n"
    "v_mfma_f32_16x16x32_bf16 v[74:77], a[200:203], v[202:205], v[74:77]\n"
    "buffer_load_dwordx4 v[154:157], v7, s[36:39], s10 offen offset:768 sc1\n"
    "s_waitcnt vmcnt(14)\n"
    "v_max3_u32 v28, v215, v217, v219\n"
    "v_mfma_f32_16x16x32_bf16 v[86:89], a[204:207], v[198:201], v[86:89]\n"
    "v_max3_u32 v28, v28, v221, v223\n"
    "v_mfma_f32_16x16x32_bf16 v[90:93], a[204:207], v[202:205], v[90:93]\n"
    "v_max3_u32 v28, v28, v225, v227\n"
    "v_mfma_f32_16x16x32_bf16 v[54:57], a[212:215], v[206:209], v[54:57]\n"
    "buffer_load_dwordx4 v[158:161], v7, s[36:39], s11 offen offset:512 sc1\n"
    "v_max_u32 v28, v28, v229\n"
    "v_mfma_f32_16x16x32_bf16 v[58:61], a[212:215], v[210:213], v[58:61]\n"
    "v_cmp_eq_u32 vcc, 0xffbfffbf, v28\n"
    "v_mfma_f32_16x16x32_bf16 v[70:73], a[216:219], v[206:209], v[70:73]\n"
    "s_cbranch_vccnz .Lk_slow_28\n"
    ".Lk_back_29:\n"
    "v_mfma_f32_16x16x32_bf16 v[74:77], a[216:219], v[210:213], v[74:77]\n"
    "buffer_load_dwordx4 v[162:165], v7, s[36:39], s11 offen offset:768 sc1\n"
    "v_mfma_f32_16x16x32_bf16 v[86:89], a[220:223], v[206:209], v[86:89]\n"
    "v_mfma_f32_16x16x32_bf16 v[90:93], a[220:223], v[210:213], v[90:93]\n"
    "s_waitcnt lgkmcnt(12)\n"
    "v_mfma_f32_16x16x32_bf16 v[54:57], a[228:231], v[214:217], v[54:57]\n"
    "buffer_load_dwordx4 v[166:169], v7, s[36:39], s12 offen offset:512 sc1\n"
    "v_mfma_f32_16x16x32_bf16 v[58:61], a[228:231], v[218:221], v[58:61]\n"
    "s_waitcnt lgkmcnt(4)\n"
    "v_mfma_f32_16x16x32_bf16 v[54:57], a[244:247], v[222:225], v[54:57]\n"
    "v_mfma_f32_16x16x32_bf16 v[58:61], a[244:247], v[226:229], v[58:61]\n"
    "v_mfma_f32_16x16x32_bf16 v[70:73], a[232:235], v[214:217], v[70:73]\n"
    "v_mfma_f32_16x16x32_bf16 v[74:77], a[232:235], v[218:221], v[74:77]\n"
    "buffer_load_dwordx4 v[170:173], v7, s[36:39], s12 offen offset:768 sc1\n"
    "s_waitcnt lgkmcnt(2)\n"
    "v_mfma_f32_16x16x32_bf16 v[70:73], a[248:251], v[222:225], v[70:73]\n"
    "v_mfma_f32_16x16x32_bf16 v[74:77], a[248:251], v[226:229], v[74:77]\n"
    "s_waitcnt vmcnt(14)\n"
    "v_max3_u32 v28, v103, v105, v107\n"
    "v_mfma_f32_16x16x32_bf16 v[86:89], a[236:239], v[214:217], v[86:89]\n"
    "v_max3_u32 v28, v28, v109, v111\n"
    "v_mfma_f32_16x16x32_bf16 v[90:93], a[236:239], v[218:221], v[90:93]\n"
    "v_max3_u32 v28, v28, v113, v115\n"
    "ds_write_b128 v29, v[54:57] offset:28672\n"
    "s_waitcnt lgkmcnt(1)\n"
    "v_mfma_f32_16x16x32_bf16 v[86:89], a[252:255], v[222:225], v[86:89]\n"
    "buffer_load_dwordx4 v[174:177], v7, s[36:39], s13 offen offset:512 sc1\n"
    "v_max_u32_e64 v28, v28, v117\n"
    "ds_write_b128 v29, v[58:61] offset:29696\n"
    "v_mfma_f32_16x16x32_bf16 v[90:93], a[252:255], v[226:229], v[90:93]\n"
    "v_cmp_eq_u32 vcc, 0xffbfffbf, v28\n"
    "v_mfma_f32_16x16x32_bf16 v[12:15], a[192:195], v[198:201], v[12:15]\n"
    "s_cbranch_vccnz .Lk_slow_31\n"
    ".Lk_back_32:\n"
    "v_mfma_f32_16x16x32_bf16 v[42:45], a[192:195], v[202:205], v[42:45]\n"
    "ds_write_b128 v29, v[70:73] offset:32768\n"
    "v_mfma_f32_16x16x32_bf16 v[12:15], a[208:211], v[206:209], v[12:15]\n"
    "ds_write_b128 v29, v[74:77] offset:33792\n"
    "v_mfma_f32_16x16x32_bf16 v[42:45], a[208:211], v[210:213], v[42:45]\n"
    "buffer_load_dwordx4 v[178:181], v7, s[36:39], s13 offen offset:768 sc1\n"
    "v_mfma_f32_16x16x32_bf16 v[12:15], a[224:227], v[214:217], v[12:15]\n"
    "v_mfma_f32_16x16x32_bf16 v[42:45], a[224:227], v[218:221], v[42:45]\n"
    "ds_write_b128 v29, v[86:89] offset:36864\n"
    "v_mfma_f32_16x16x32_bf16 v[12:15], a[240:243], v[222:225], v[12:15]\n"
    "ds_write_b128 v29, v[90:93] offset:37888\n"
    "v_mfma_f32_16x16x32_bf16 v[42:45], a[240:243], v[226:229], v[42:45]\n"
    "; ---- epilogue of the first half\n"
    "s_waitcnt lgkmcnt(0)\n"
    "s_barrier\n"
    "ds_read_b128 v[54:57], v34 offset:28672\n"
    "buffer_load_dwordx4 v[182:185], v7, s[36:39], s14 offen offset:512 sc1\n"
    "ds_read_b128 v[70:73], v35 offset:32768\n"
    "ds_read_b128 v[86:89], v36 offset:36864\n"
    "ds_read_b128 v[58:61], v34 offset:29696\n"
    "ds_read_b128 v[74:77], v35 offset:33792\n"
    "ds_read_b128 v[90:93], v36 offset:37888\n"
    "s_waitcnt lgkmcnt(5)\n"
    "v_pk_add_f32 v[12:13], v[12:13], v[54:55]\n"
    "v_pk_add_f32 v[14:15], v[14:15], v[56:57]\n"
    "s_waitcnt lgkmcnt(3)\n"
    "v_pk_add_f32 v[70:71], v[70:71], v[86:87]\n"
    "v_pk_add_f32 v[72:73], v[72:73], v[88:89]\n"
    "v_pk_add_f32 v[12:13], v[12:13], v[70:71]\n"
    "v_pk_add_f32 v[14:15], v[14:15], v[72:73]\n"
    "s_waitcnt lgkmcnt(2)\n"
    "v_pk_add_f32 v[42:43], v[42:43], v[58:59]\n"
    "v_pk_add_f32 v[44:45], v[44:45], v[60:61]\n"
    "buffer_load_dwordx4 v[186:189], v7, s[36:39], s14 offen offset:768 sc1\n"
    "s_waitcnt lgkmcnt(0)\n"
    "v_pk_add_f32 v[74:75], v[74:75], v[90:91]\n"
    "v_pk_add_f32 v[76:77], v[76:77], v[92:93]\n"
    "v_pk_add_f32 v[42:43], v[42:43], v[74:75]\n"
    "v_pk_add_f32 v[44:45], v[44:45], v[76:77]\n"
    "v_pk_mul_f32 v[54:55], v[12:13], v[12:13]\n"
    "v_pk_mul_f32 v[56:57], v[14:15], v[14:15]\n"
    "v_pk_mul_f32 v[58:59], v[42:43], v[42:43]\n"
    "v_pk_mul_f32 v[60:61], v[44:45], v[44:45]\n"
    "v_pk_fma_f32 v[54:55], v[54:55], s[74:75], v[240:241] op_sel_hi:[1,0,0]\n"
    "v_pk_fma_f32 v[56:57], v[56:57], s[74:75], v[240:241] op_sel_hi:[1,0,0]\n"
    "v_pk_fma_f32 v[58:59], v[58:59], s[74:75], v[240:241] op_sel_hi:[1,0,0]\n"
    "v_pk_fma_f32 v[60:61], v[60:61], s[74:75], v[240:241] op_sel_hi:[1,0,0]\n"
    "v_pk_mul_f32 v[54:55], v[54:55], v[12:13]\n"
    "v_pk_mul_f32 v[56:57], v[56:57], v[14:15]\n"
    "v_pk_mul_f32 v[58:59], v[58:59], v[42:43]\n"
    "v_pk_mul_f32 v[60:61], v[60:61], v[44:45]\n"
    "v_exp_f32 v54, v54\n"
    "buffer_load_dwordx4 v[190:193], v7, s[36:39], s15 offen offset:512 sc1\n"
    "v_exp_f32 v55, v55\n"
    "v_exp_f32 v56, v56\n"
    "v_exp_f32 v57, v57\n"
    "v_exp_f32 v58, v58\n"
    "v_exp_f32 v59, v59\n"
    "v_exp_f32 v60, v60\n"
    "v_exp_f32 v61, v61\n"
    "v_pk_add_f32 v[54:55], v[54:55], 1.0 op_sel_hi:[1,0]\n"
    "v_pk_add_f32 v[56:57], v[56:57], 1.0 op_sel_hi:[1,0]\n"
    "v_pk_add_f32 v[58:59], v[58:59], 1.0 op_sel_hi:[1,0]\n"
    "v_pk_add_f32 v[60:61], v[60:61], 1.0 op_sel_hi:[1,0]\n"
    "buffer_load_dwordx4 v[194:197], v7, s[36:39], s15 offen offset:768 sc1\n"
    "v_rcp_f32 v54, v54\n"
    "v_rcp_f32 v55, v55\n"
    "v_rcp_f32 v56, v56\n"
    "v_rcp_f32 v57, v57\n"
    "v_rcp_f32 v58, v58\n"
    "v_rcp_f32 v59, v59\n"
    "v_rcp_f32 v60, v60\n"
    "v_rcp_f32 v61, v61\n"
    "v_pk_mul_f32 v[12:13], v[12:13], v[54:55]\n"
    "v_pk_mul_f32 v[14:15], v[14:15], v[56:57]\n"
    "v_pk_mul_f32 v[42:43], v[42:43], v[58:59]\n"
    "v_pk_mul_f32 v[44:45], v[44:45], v[60:61]\n"
    "v_cvt_pk_bf16_f32 v70, v12, v13\n"
    "v_cvt_pk_bf16_f32 v72, v42, v43\n"
    "v_cvt_pk_bf16_f32 v71, v14, v15\n"
    "v_cvt_pk_bf16_f32 v73, v44, v45\n"
    "v_permlane16_swap_b32 v70, v72\n"
    "v_mov_b32 v86, 0xffbfffbf\n"
    "v_permlane16_swap_b32 v71, v73\n"
    "v_mov_b32 v87, 0xffbfffbf\n"
    "v_mov_b32 v88, 0xffbfffbf\n"
    "v_mov_b32 v89, 0xffbfffbf\n"
    "buffer_store_dwordx4 v[70:73], v24, s[36:39], s80 offen offset:0 sc1\n"
    "buffer_store_dwordx4 v[86:89], v24, s[36:39], s80 offen offset:512 sc1\n"
    "; ---- second half\n"
    "v_mfma_f32_16x16x32_bf16 v[62:65], a[4:7], v[102:105], 0\n"
    "v_mfma_f32_16x16x32_bf16 v[66:69], a[4:7], v[106:109], 0\n"
    "v_mfma_f32_16x16x32_bf16 v[78:81], a[8:11], v[102:105], 0\n"
    "v_mfma_f32_16x16x32_bf16 v[82:85], a[8:11], v[106:109], 0\n"
    "v_mfma_f32_16x16x32_bf16 v[94:97], a[12:15], v[102:105], 0\n"
    "v_mfma_f32_16x16x32_bf16 v[98:101], a[12:15], v[106:109], 0\n"
    "v_mfma_f32_16x16x32_bf16 v[46:49], a[0:3], v[102:105], v[30:33]\n"
    "v_mfma_f32_16x16x32_bf16 v[50:53], a[0:3], v[106:109], v[30:33]\n"
    "s_waitcnt vmcnt(18)\n"
    "v_max3_u32 v28, v119, v121, v123\n"
    "v_mfma_f32_16x16x32_bf16 v[62:65], a[20:23], v[110:113], v[62:65]\n"
    "v_max3_u32 v28, v28, v125, v127\n"
    "v_mfma_f32_16x16x32_bf16 v[66:69], a[20:23], v[114:117], v[66:69]\n"
    "v_max3_u32 v28, v28, v129, v131\n"
    "v_mfma_f32_16x16x32_bf16 v[78:81], a[24:27], v[110:113], v[78:81]\n"
    "v_max_u32_e64 v28, v28, v133\n"
    "v_mfma_f32_16x16x32_bf16 v[82:85], a[24:27], v[114:117], v[82:85]\n"
    "v_cmp_eq_u32 vcc, 0xffbfffbf, v28\n"
    "v_mfma_f32_16x16x32_bf16 v[94:97], a[28:31], v[110:113], v[94:97]\n"
    "s_cbranch_vccnz .Lk_slow_34\n"
    ".Lk_back_35:\n"
    "v_mfma_f32_16x16x32_bf16 v[98:101], a[28:31], v[114:117], v[98:101]\n"
    "v_mfma_f32_16x16x32_bf16 v[46:49], a[16:19], v[110:113], v[46:49]\n"
    "v_mfma_f32_16x16x32_bf16 v[50:53], a[16:19], v[114:117], v[50:53]\n"
    "v_mfma_f32_16x16x32_bf16 v[62:65], a[36:39], v[118:121], v[62:65]\n"
    "buffer_load_dwordx4 v[198:201], v7, s[36:39], s16 offen offset:512 sc1\n"
    "v_mfma_f32_16x16x32_bf16 v[66:69], a[36:39], v[122:125], v[66:69]\n"
    "ds_read_b64_tr_b16 a[0:1], v20 offset:0\n"
    "v_mfma_f32_16x16x32_bf16 v[78:81], a[40:43], v[118:121], v[78:81]\n"
    "ds_read_b64_tr_b16 a[2:3], v20 offset:512\n"
    "v_mfma_f32_16x16x32_bf16 v[82:85], a[40:43], v[122:125], v[82:85]\n"
    "ds_read_b64_tr_b16 a[4:5], v21 offset:0\n"
    "v_mfma_f32_16x16x32_bf16 v[94:97], a[44:47], v[118:121], v[94:97]\n"
    "buffer_load_dwordx4 v[202:205], v7, s[36:39], s16 offen offset:768 sc1\n"
    "v_mfma_f32_16x16x32_bf16 v[98:101], a[44:47], v[122:125], v[98:101]\n"
    "ds_read_b64_tr_b16 a[6:7], v21 offset:512\n"
    "v_mfma_f32_16x16x32_bf16 v[46:49], a[32:35], v[118:121], v[46:49]\n"
    "ds_read_b64_tr_b16 a[8:9], v22 offset:0\n"
    "v_mfma_f32_16x16x32_bf16 v[50:53], a[32:35], v[122:125], v[50:53]\n"
    "s_waitcnt vmcnt(16)\n"
    "v_max3_u32 v28, v135, v137, v139\n"
    "v_mfma_f32_16x16x32_bf16 v[62:65], a[52:55], v[126:129], v[62:65]\n"
    "buffer_load_dwordx4 v[206:209], v7, s[36:39], s17 offen offset:512 sc1\n"
    "v_max3_u32 v28, v28, v141, v143\n"
    "v_mfma_f32_16x16x32_bf16 v[66:69], a[52:55], v[130:133], v[66:69]\n"
    "v_max3_u32 v28, v28, v145, v147\n"
    "v_mfma_f32_16x16x32_bf16 v[78:81], a[56:59], v[126:129], v[78:81]\n"
    "v_max_u32_e64 v28, v28, v149\n"
    "v_mfma_f32_16x16x32_bf16 v[82:85], a[56:59], v[130:133], v[82:85]\n"
    "v_cmp_eq_u32 vcc, 0xffbfffbf, v28\n"
    "v_mfma_f32_16x16x32_bf16 v[94:97], a[60:63], v[126:129], v[94:97]\n"
    "buffer_load_dwordx4 v[210:213], v7, s[36:39], s17 offen offset:768 sc1\n"
    "v_mfma_f32_16x16x32_bf16 v[98:101], a[60:63], v[130:133], v[98:101]\n"
    "s_cbranch_vccnz .Lk_slow_37\n"
    ".Lk_back_38:\n"
    "v_mfma_f32_16x16x32_bf16 v[46:49], a[48:51], v[126:129], v[46:49]\n"
    "ds_read_b64_tr_b16 a[10:11], v22 offset:512\n"
    "v_mfma_f32_16x16x32_bf16 v[50:53], a[48:51], v[130:133], v[50:53]\n"
    "ds_read_b64_tr_b16 a[12:13], v23 offset:0\n"
    "v_mfma_f32_16x16x32_bf16 v[62:65], a[68:71], v[134:137], v[62:65]\n"
    "buffer_load_dwordx4 v[214:217], v7, s[36:39], s18 offen offset:512 sc1\n"
    "v_mfma_f32_16x16x32_bf16 v[66:69], a[68:71], v[138:141], v[66:69]\n"
    "ds_read_b64_tr_b16 a[14:15], v23 offset:512\n"
    "v_mfma_f32_16x16x32_bf16 v[78:81], a[72:75], v[134:137], v[78:81]\n"
    "ds_read_b64_tr_b16 a[16:17], v20 offset:16384\n"
    "v_mfma_f32_16x16x32_bf16 v[82:85], a[72:75], v[138:141], v[82:85]\n"
    "ds_read_b64_tr_b16 a[18:19], v20 offset:16896\n"
    "v_mfma_f32_16x16x32_bf16 v[94:97], a[76:79], v[134:137], v[94:97]\n"
    "buffer_load_dwordx4 v[218:221], v7, s[36:39], s18 offen offset:768 sc1\n"
    "v_mfma_f32_16x16x32_bf16 v[98:101], a[76:79], v[138:141], v[98:101]\n"
    "ds_read_b64_tr_b16 a[20:21], v21 offset:16384\n"
    "v_mfma_f32_16x16x32_bf16 v[46:49], a[64:67], v[134:137], v[46:49]\n"
    "ds_read_b64_tr_b16 a[22:23], v21 offset:16896\n"
    "v_mfma_f32_16x16x32_bf16 v[50:53], a[64:67], v[138:141], v[50:53]\n"
    "s_waitcnt vmcnt(16)\n"
    "v_max3_u32 v28, v151, v153, v155\n"
    "v_mfma_f32_16x16x32_bf16 v[62:65], a[84:87], v[142:145], v[62:65]\n"
    "buffer_load_dwordx4 v[222:225], v7, s[36:39], s19 offen offset:512 sc1\n"
    "v_max3_u32 v28, v28, v157, v159\n"
    "v_mfma_f32_16x16x32_bf16 v[66:69], a[84:87], v[146:149], v[66:69]\n"
    "v_max3_u32 v28, v28, v161, v163\n"
    "v_mfma_f32_16x16x32_bf16 v[78:81], a[88:91], v[142:145], v[78:81]\n"
    "v_max_u32_e64 v28, v28, v165\n"
    "v_mfma_f32_16x16x32_bf16 v[82:85], a[88:91], v[146:149], v[82:85]\n"
    "v_cmp_eq_u32 vcc, 0xffbfffbf, v28\n"
    "v_mfma_f32_16x16x32_bf16 v[94:97], a[92:95], v[142:145], v[94:97]\n"
    "buffer_load_dwordx4 v[226:229], v7, s[36:39], s19 offen offset:768 sc1\n"
    "v_mfma_f32_16x16x32_bf16 v[98:101], a[92:95], v[146:149], v[98:101]\n"
    "s_cbranch_vccnz .Lk_slow_40\n"
    ".Lk_back_41:\n"
    "v_mfma_f32_16x16x32_bf16 v[46:49], a[80:83], v[142:145], v[46:49]\n"
    "ds_read_b64_tr_b16 a[24:25], v22 offset:16384\n"
    "v_mfma_f32_16x16x32_bf16 v[50:53], a[80:83], v[146:149], v[50:53]\n"
    "ds_read_b64_tr_b16 a[26:27], v22 offset:16896\n"
    "v_mfma_f32_16x16x32_bf16 v[62:65], a[100:103], v[150:153], v[62:65]\n"
    "buffer_load_dwordx4 v[230:233], v38, s[36:39], s85 offen offset:0 sc1\n"
    "v_mfma_f32_16x16x32_bf16 v[66:69], a[100:103], v[154:157], v[66:69]\n"
    "v_mfma_f32_16x16x32_bf16 v[78:81], a[104:107], v[150:153], v[78:81]\n"
    "v_mfma_f32_16x16x32_bf16 v[82:85], a[104:107], v[154:157], v[82:85]\n"
    "buffer_load_dwordx4 v[234:237], v38, s[36:39], s85 offen offset:512 sc1\n"
    "v_mfma_f32_16x16x32_bf16 v[94:97], a[108:111], v[150:153], v[94:97]\n"
    "v_mfma_f32_16x16x32_bf16 v[98:101], a[108:111], v[154:157], v[98:101]\n"
    "v_mfma_f32_16x16x32_bf16 v[46:49], a[96:99], v[150:153], v[46:49]\n"
    "buffer_load_dwordx2 v[26:27], v39, s[36:39], s86 offen offset:0 sc1\n"
    "v_mfma_f32_16x16x32_bf16 v[50:53], a[96:99], v[154:157], v[50:53]\n"
    "s_waitcnt vmcnt(17)\n"
    "v_max3_u32 v28, v167, v169, v171\n"
    "v_mfma_f32_16x16x32_bf16 v[62:65], a[116:119], v[158:161], v[62:65]\n"
    "v_max3_u32 v28, v28, v173, v175\n"
    "v_mfma_f32_16x16x32_bf16 v[66:69], a[116:119], v[162:165], v[66:69]\n"
    "v_max3_u32 v28, v28, v177, v179\n"
    "v_mfma_f32_16x16x32_bf16 v[78:81], a[120:123], v[158:161], v[78:81]\n"
    "v_max_u32_e64 v28, v28, v181\n"
    "v_mfma_f32_16x16x32_bf16 v[82:85], a[120:123], v[162:165], v[82:85]\n"
    "v_cmp_eq_u32 vcc, 0xffbfffbf, v28\n"
    "v_mfma_f32_16x16x32_bf16 v[94:97], a[124:127], v[158:161], v[94:97]\n"
    "s_cbranch_vccnz .Lk_slow_43\n"
    ".Lk_back_44:\n"
    "v_mfma_f32_16x16x32_bf16 v[98:101], a[124:127], v[162:165], v[98:101]\n"
    "ds_read_b64_tr_b16 a[28:29], v23 offset:16384\n"
    "v_mfma_f32_16x16x32_bf16 v[46:49], a[112:115], v[158:161], v[46:49]\n"
    "ds_read_b64_tr_b16 a[30:31], v23 offset:16896\n"
    "v_mfma_f32_16x16x32_bf16 v[50:53], a[112:115], v[162:165], v[50:53]\n"
    "ds_read_b64_tr_b16 a[32:33], v20 offset:20480\n"
    "v_mfma_f32_16x16x32_bf16 v[62:65], a[132:135], v[166:169], v[62:65]\n"
    "ds_read_b64_tr_b16 a[34:35], v20 offset:20992\n"
    "v_mfma_f32_16x16x32_bf16 v[66:69], a[132:135], v[170:173], v[66:69]\n"
    "ds_read_b64_tr_b16 a[36:37], v21 offset:20480\n"
    "v_mfma_f32_16x16x32_bf16 v[78:81], a[136:139], v[166:169], v[78:81]\n"
    "ds_read_b64_tr_b16 a[38:39], v21 offset:20992\n"
    "v_mfma_f32_16x16x32_bf16 v[82:85], a[136:139], v[170:173], v[82:85]\n"
    "ds_read_b64_tr_b16 a[40:41], v22 offset:20480\n"
    "v_mfma_f32_16x16x32_bf16 v[94:97], a[140:143], v[166:169], v[94:97]\n"
    "ds_read_b64_tr_b16 a[42:43], v22 offset:20992\n"
    "v_mfma_f32_16x16x32_bf16 v[98:101], a[140:143], v[170:173], v[98:101]\n"
    "ds_read_b64_tr_b16 a[44:45], v23 offset:20480\n"
    "v_mfma_f32_16x16x32_bf16 v[46:49], a[128:131], v[166:169], v[46:49]\n"
    "ds_read_b64_tr_b16 a[46:47], v23 offset:20992\n"
    "v_mfma_f32_16x16x32_bf16 v[50:53], a[128:131], v[170:173], v[50:53]\n"
    "s_waitcnt vmcnt(13)\n"
    "v_max3_u32 v28, v183, v185, v187\n"
    "v_mfma_f32_16x16x32_bf16 v[62:65], a[148:151], v[174:177], v[62:65]\n"
    "v_max3_u32 v28, v28, v189, v191\n"
    "v_mfma_f32_16x16x32_bf16 v[66:69], a[148:151], v[178:181], v[66:69]\n"
    "v_max3_u32 v28, v28, v193, v195\n"
    "v_mfma_f32_16x16x32_bf16 v[78:81], a[152:155], v[174:177], v[78:81]\n"
    "v_max_u32_e64 v28, v28, v197\n"
    "v_mfma_f32_16x16x32_bf16 v[82:85], a[152:155], v[178:181], v[82:85]\n"
    "v_cmp_eq_u32 vcc, 0xffbfffbf, v28\n"
    "v_mfma_f32_16x16x32_bf16 v[94:97], a[156:159], v[174:177], v[94:97]\n"
    "s_cbranch_vccnz .Lk_slow_46\n"
    ".Lk_back_47:\n"
    "v_mfma_f32_16x16x32_bf16 v[98:101], a[156:159], v[178:181], v[98:101]\n"
    "ds_read_b64_tr_b16 a[48:49], v20 offset:4096\n"
    "v_mfma_f32_16x16x32_bf16 v[46:49], a[144:147], v[174:177], v[46:49]\n"
    "ds_read_b64_tr_b16 a[50:51], v20 offset:4608\n"
    "v_mfma_f32_16x16x32_bf16 v[50:53], a[144:147], v[178:181], v[50:53]\n"
    "ds_read_b64_tr_b16 a[52:53], v21 offset:4096\n"
    "v_mfma_f32_16x16x32_bf16 v[62:65], a[164:167], v[182:185], v[62:65]\n"
    "s_add_u32 m0, s62, 0x2000\n"
    "s_add_u32 s71, s64, 0x20000\n"
    "s_add_u32 s72, s64, 0xffe60000\n"
    "s_min_u32 s71, s71, s72\n"
    "buffer_load_dwordx4 v16, s[44:47], s71 offen offset:0 lds\n"
    "v_mfma_f32_16x16x32_bf16 v[66:69], a[164:167], v[186:189], v[66:69]\n"
    "v_mov_b32 v238, s88\n"
    "v_mfma_f32_16x16x32_bf16 v[78:81], a[168:171], v[182:185], v[78:81]\n"
    "v_mov_b32_e64 v239, s89\n"
    "v_mfma_f32_16x16x32_bf16 v[82:85], a[168:171], v[186:189], v[82:85]\n"
    "v_lshl_add_u32 v8, v4, 24, s61\n"
    "v_mfma_f32_16x16x32_bf16 v[94:97], a[172:175], v[182:185], v[94:97]\n"
    "s_waitcnt vmcnt(12)\n"
    "buffer_store_dwordx2 v[238:239], v8, s[36:39], s87 offen offset:0 sc1\n"
    "v_mfma_f32_16x16x32_bf16 v[98:101], a[172:175], v[186:189], v[98:101]\n"
    "ds_read_b64_tr_b16 a[54:55], v21 offset:4608\n"
    "v_mfma_f32_16x16x32_bf16 v[46:49], a[160:163], v[182:185], v[46:49]\n"
    "ds_read_b64_tr_b16 a[56:57], v22 offset:4096\n"
    "v_mfma_f32_16x16x32_bf16 v[50:53], a[160:163], v[186:189], v[50:53]\n"
    "s_waitcnt vmcnt(9)\n"
    "v_max3_u32 v28, v199, v201, v203\n"
    "v_mfma_f32_16x16x32_bf16 v[62:65], a[180:183], v[190:193], v[62:65]\n"
    "buffer_load_dwordx4 v17, s[44:47], s71 offen offset:1024 lds\n"
    "v_max3_u32 v28, v28, v205, v207\n"
    "v_mfma_f32_16x16x32_bf16 v[66:69], a[180:183], v[194:197], v[66:69]\n"
    "v_max3_u32 v28, v28, v209, v211\n"
    "v_mfma_f32_16x16x32_bf16 v[78:81], a[184:187], v[190:193], v[78:81]\n"
    "v_max_u32 v28, v28, v213\n"
    "v_mfma_f32_16x16x32_bf16 v[82:85], a[184:187], v[194:197], v[82:85]\n"
    "v_cmp_eq_u32 vcc, 0xffbfffbf, v28\n"
    "v_mfma_f32_16x16x32_bf16 v[94:97], a[188:191], v[190:193], v[94:97]\n"
    "s_cbranch_vccnz .Lk_slow_49\n"
    ".Lk_back_50:\n"
    "v_mfma_f32_16x16x32_bf16 v[98:101], a[188:191], v[194:197], v[98:101]\n"
    "ds_read_b64_tr_b16 a[58:59], v22 offset:4608\n"
    "v_mfma_f32_16x16x32_bf16 v[46:49], a[176:179], v[190:193], v[46:49]\n"
    "ds_read_b64_tr_b16 a[60:61], v23 offset:4096\n"
    "v_mfma_f32_16x16x32_bf16 v[50:53], a[176:179], v[194:197], v[50:53]\n"
    "ds_read_b64_tr_b16 a[62:63], v23 offset:4608\n"
    "v_mfma_f32_16x16x32_bf16 v[62:65], a[196:199], v[198:201], v[62:65]\n"
    "buffer_load_dwordx4 v18, s[44:47], s71 offen offset:2048 lds\n"
    "v_mfma_f32_16x16x32_bf16 v[66:69], a[196:199], v[202:205], v[66:69]\n"
    "ds_read_b64_tr_b16 a[64:65], v20 offset:24576\n"
    "v_mfma_f32_16x16x32_bf16 v[78:81], a[200:203], v[198:201], v[78:81]\n"
    "ds_read_b64_tr_b16 a[66:67], v20 offset:25088\n"
    "v_mfma_f32_16x16x32_bf16 v[82:85], a[200:203], v[202:205], v[82:85]\n"
    "buffer_load_dwordx4 v19, s[44:47], s71 offen offset:3072 lds\n"
    "s_waitcnt vmcnt(8)\n"
    "v_max3_u32 v28, v215, v217, v219\n"
    "v_mfma_f32_16x16x32_bf16 v[94:97], a[204:207], v[198:201], v[94:97]\n"
    "v_max3_u32 v28, v28, v221, v223\n"
    "v_mfma_f32_16x16x32_bf16 v[98:101], a[204:207], v[202:205], v[98:101]\n"
    "v_max3_u32 v28, v28, v225, v227\n"
    "v_mfma_f32_16x16x32_bf16 v[62:65], a[212:215], v[206:209], v[62:65]\n"
    "s_add_u32 m0, s62, 0x3000\n"
    "s_add_u32 s71, s64, 0x140000\n"
    "s_add_u32 s72, s64, 0xfff80000\n"
    "s_min_u32 s71, s71, s72\n"
    "buffer_load_dwordx4 v16, s[44:47], s71 offen offset:0 lds\n"
    "v_max_u32 v28, v28, v229\n"
    "v_mfma_f32_16x16x32_bf16 v[66:69], a[212:215], v[210:213], v[66:69]\n"
    "v_cmp_eq_u32 vcc, 0xffbfffbf, v28\n"
    "v_mfma_f32_16x16x32_bf16 v[78:81], a[216:219], v[206:209], v[78:81]\n"
    "s_cbranch_vccnz .Lk_slow_52\n"
    ".Lk_back_53:\n"
    "v_mfma_f32_16x16x32_bf16 v[82:85], a[216:219], v[210:213], v[82:85]\n"
    "buffer_load_dwordx4 v17, s[44:47], s71 offen offset:1024 lds\n"
    "v_mfma_f32_16x16x32_bf16 v[94:97], a[220:223], v[206:209], v[94:97]\n"
    "ds_read_b64_tr_b16 a[68:69], v21 offset:24576\n"
    "v_mfma_f32_16x16x32_bf16 v[98:101], a[220:223], v[210:213], v[98:101]\n"
    "ds_read_b64_tr_b16 a[70:71], v21 offset:25088\n"
    "v_mfma_f32_16x16x32_bf16 v[62:65], a[228:231], v[214:217], v[62:65]\n"
    "buffer_load_dwordx4 v18, s[44:47], s71 offen offset:2048 lds\n"
    "v_mfma_f32_16x16x32_bf16 v[66:69], a[228:231], v[218:221], v[66:69]\n"
    "ds_read_b64_tr_b16 a[72:73], v22 offset:24576\n"
    "v_mfma_f32_16x16x32_bf16 v[62:65], a[244:247], v[222:225], v[62:65]\n"
    "ds_read_b64_tr_b16 a[74:75], v22 offset:25088\n"
    "v_mfma_f32_16x16x32_bf16 v[66:69], a[244:247], v[226:229], v[66:69]\n"
    "ds_read_b64_tr_b16 a[76:77], v23 offset:24576\n"
    "v_mfma_f32_16x16x32_bf16 v[78:81], a[232:235], v[214:217], v[78:81]\n"
    "ds_read_b64_tr_b16 a[78:79], v23 offset:25088\n"
    "v_mfma_f32_16x16x32_bf16 v[82:85], a[232:235], v[218:221], v[82:85]\n"
    "buffer_load_dwordx4 v19, s[44:47], s71 offen offset:3072 lds\n"
    "v_mfma_f32_16x16x32_bf16 v[78:81], a[248:251], v[222:225], v[78:81]\n"
    "v_mfma_f32_16x16x32_bf16 v[82:85], a[248:251], v[226:229], v[82:85]\n"
    "v_mfma_f32_16x16x32_bf16 v[94:97], a[236:239], v[214:217], v[94:97]\n"
    "v_mfma_f32_16x16x32_bf16 v[98:101], a[236:239], v[218:221], v[98:101]\n"
    "ds_write_b128 v29, v[62:65] offset:30720\n"
    "v_mfma_f32_16x16x32_bf16 v[94:97], a[252:255], v[222:225], v[94:97]\n"
    "s_waitcnt lgkmcnt(15)\n"
    "s_add_u32 m0, s62, 0\n"
    "s_add_u32 s71, s64, 0x180000\n"
    "s_add_u32 s72, s64, 0xfffc0000\n"
    "s_min_u32 s71, s71, s72\n"
    "s_nop 0\n"
    "buffer_load_dwordx4 v16, s[44:47], s71 offen offset:0 lds\n"
    "ds_write_b128 v29, v[66:69] offset:31744\n"
    "v_mfma_f32_16x16x32_bf16 v[98:101], a[252:255], v[226:229], v[98:101]\n"
    "v_mfma_f32_16x16x32_bf16 v[46:49], a[192:195], v[198:201], v[46:49]\n"
    "v_mfma_f32_16x16x32_bf16 v[50:53], a[192:195], v[202:205], v[50:53]\n"
    "ds_write_b128 v29, v[78:81] offset:34816\n"
    "v_mfma_f32_16x16x32_bf16 v[46:49], a[208:211], v[206:209], v[46:49]\n"
    "ds_write_b128 v29, v[82:85] offset:35840\n"
    "v_mfma_f32_16x16x32_bf16 v[50:53], a[208:211], v[210:213], v[50:53]\n"
    "buffer_load_dwordx4 v17, s[44:47], s71 offen offset:1024 lds\n"
    "v_mfma_f32_16x16x32_bf16 v[46:49], a[224:227], v[214:217], v[46:49]\n"
    "v_mfma_f32_16x16x32_bf16 v[50:53], a[224:227], v[218:221], v[50:53]\n"
    "ds_write_b128 v29, v[94:97] offset:38912\n"
    "v_mfma_f32_16x16x32_bf16 v[46:49], a[240:243], v[222:225], v[46:49]\n"
    "ds_write_b128 v29, v[98:101] offset:39936\n"
    "v_mfma_f32_16x16x32_bf16 v[50:53], a[240:243], v[226:229], v[50:53]\n"
    "; ---- epilogue of the second half\n"
    "s_waitcnt lgkmcnt(0)\n"
    "s_barrier\n"
    "ds_read_b128 v[62:65], v34 offset:30720\n"
    "buffer_load_dwordx4 v18, s[44:47], s71 offen offset:2048 lds\n"
    "ds_read_b128 v[78:81], v35 offset:34816\n"
    "ds_read_b128 v[94:97], v36 offset:38912\n"
    "ds_read_b128 v[66:69], v34 offset:31744\n"
    "ds_read_b128 v[82:85], v35 offset:35840\n"
    "ds_read_b128 v[98:101], v36 offset:39936\n"
    "s_waitcnt lgkmcnt(5)\n"
    "v_pk_add_f32 v[46:47], v[46:47], v[62:63]\n"
    "v_pk_add_f32 v[48:49], v[48:49], v[64:65]\n"
    "s_waitcnt lgkmcnt(3)\n"
    "v_pk_add_f32 v[78:79], v[78:79], v[94:95]\n"
    "buffer_load_dwordx4 v19, s[44:47], s71 offen offset:3072 lds\n"
    "v_pk_add_f32 v[80:81], v[80:81], v[96:97]\n"
    "v_pk_add_f32 v[46:47], v[46:47], v[78:79]\n"
    "v_pk_add_f32 v[48:49], v[48:49], v[80:81]\n"
    "s_waitcnt lgkmcnt(2)\n"
    "v_pk_add_f32 v[50:51], v[50:51], v[66:67]\n"
    "v_pk_add_f32 v[52:53], v[52:53], v[68:69]\n"
    "s_waitcnt lgkmcnt(0)\n"
    "v_pk_add_f32 v[82:83], v[82:83], v[98:99]\n"
    "v_pk_add_f32 v[84:85], v[84:85], v[100:101]\n"
    "v_pk_add_f32 v[50:51], v[50:51], v[82:83]\n"
    "v_pk_add_f32 v[52:53], v[52:53], v[84:85]\n"
    "v_pk_mul_f32 v[62:63], v[46:47], v[46:47]\n"
    "v_pk_mul_f32 v[64:65], v[48:49], v[48:49]\n"
    "v_pk_mul_f32 v[66:67], v[50:51], v[50:51]\n"
    "s_add_u32 m0, s62, 0x4000\n"
    "s_add_u32 s71, s64, 0x60000\n"
    "s_add_u32 s72, s64, 0xffea0000\n"
    "s_min_u32 s71, s71, s72\n"
    "s_nop 0\n"
    "buffer_load_dwordx4 v16, s[44:47], s71 offen offset:0 lds\n"
    "v_pk_mul_f32 v[68:69], v[52:53], v[52:53]\n"
    "v_pk_fma_f32 v[62:63], v[62:63], s[74:75], v[240:241] op_sel_hi:[1,0,0]\n"
    "v_pk_fma_f32 v[64:65], v[64:65], s[74:75], v[240:241] op_sel_hi:[1,0,0]\n"
    "v_pk_fma_f32 v[66:67], v[66:67], s[74:75], v[240:241] op_sel_hi:[1,0,0]\n"
    "v_pk_fma_f32 v[68:69], v[68:69], s[74:75], v[240:241] op_sel_hi:[1,0,0]\n"
    "v_pk_mul_f32 v[62:63], v[62:63], v[46:47]\n"
    "v_pk_mul_f32 v[64:65], v[64:65], v[48:49]\n"
    "v_pk_mul_f32 v[66:67], v[66:67], v[50:51]\n"
    "v_pk_mul_f32 v[68:69], v[68:69], v[52:53]\n"
    "v_exp_f32 v62, v62\n"
    "v_exp_f32 v63, v63\n"
    "buffer_load_dwordx4 v17, s[44:47], s71 offen offset:1024 lds\n"
    "v_exp_f32 v64, v64\n"
    "v_exp_f32 v65, v65\n"
    "v_exp_f32 v66, v66\n"
    "v_exp_f32 v67, v67\n"
    "v_exp_f32 v68, v68\n"
    "v_exp_f32 v69, v69\n"
    "v_pk_add_f32 v[62:63], v[62:63], 1.0 op_sel_hi:[1,0]\n"
    "buffer_load_dwordx4 v18, s[44:47], s71 offen offset:2048 lds\n"
    "v_pk_add_f32 v[64:65], v[64:65], 1.0 op_sel_hi:[1,0]\n"
    "v_pk_add_f32 v[66:67], v[66:67], 1.0 op_sel_hi:[1,0]\n"
    "v_pk_add_f32 v[68:69], v[68:69], 1.0 op_sel_hi:[1,0]\n"
    "v_rcp_f32 v62, v62\n"
    "v_rcp_f32 v63, v63\n"
    "v_rcp_f32 v64, v64\n"
    "v_rcp_f32 v65, v65\n"
    "v_rcp_f32 v66, v66\n"
    "buffer_load_dwordx4 v19, s[44:47], s71 offen offset:3072 lds\n"
    "v_rcp_f32 v67, v67\n"
    "v_rcp_f32 v68, v68\n"
    "v_rcp_f32 v69, v69\n"
    "v_pk_mul_f32 v[46:47], v[46:47], v[62:63]\n"
    "v_pk_mul_f32 v[48:49], v[48:49], v[64:65]\n"
    "v_pk_mul_f32 v[50:51], v[50:51], v[66:67]\n"
    "v_pk_mul_f32 v[52:53], v[52:53], v[68:69]\n"
    "v_cvt_pk_bf16_f32 v78, v46, v47\n"
    "v_cvt_pk_bf16_f32 v80, v50, v51\n"
    "v_cvt_pk_bf16_f32 v79, v48, v49\n"
    "v_cvt_pk_bf16_f32 v81, v52, v53\n"
    "v_permlane16_swap_b32 v78, v80\n"
    "s_nop 0\n"
    "v_permlane16_swap_b32 v79, v81\n"
    "s_nop 0\n"
    "buffer_store_dwordx4 v[78:81], v24, s[36:39], s80 offen offset:512 sc1\n"
    "; ---- residual, rest of W2\n"
    "buffer_load_dwordx2 v[238:239], v41, s[36:39], s90 offen offset:0 sc1\n"
    "s_waitcnt vmcnt(20)\n"
    "v_max3_u32 v8, v231, v233, v235\n"
    "s_waitcnt vmcnt(19)\n"
    "v_max3_u32 v8, v8, v237, v27\n"
    "v_cmp_eq_u32 vcc, 0xffbfffbf, v8\n"
    "s_nop 0\n"
    "s_cbranch_vccnz .Lk_slow_55\n"
    ".Lk_back_56:\n"
    "s_add_u32 m0, s62, 0x5000\n"
    "s_add_u32 s71, s64, 0xa0000\n"
    "s_add_u32 s72, s64, 0xffee0000\n"
    "s_min_u32 s71, s71, s72\n"
    "buffer_load_dwordx4 v16, s[44:47], s71 offen offset:0 lds\n"
    "v_lshlrev_b32 v8, 3, v5\n"
    "v_sub_u32 v8, v6, v8\n"
    "v_and_b32 v9, 1, v6\n"
    "v_lshlrev_b32 v9, 4, v9\n"
    "s_movk_i32 s55, 0x3f80\n"
    "v_lshlrev_b32_e64 v9, v9, s55\n"
    "v_ashrrev_i32 v8, 1, v8\n"
    "v_cmp_eq_u32 vcc, 0, v8\n"
    "buffer_load_dwordx4 v17, s[44:47], s71 offen offset:1024 lds\n"
    "s_nop 0\n"
    "v_cndmask_b32 v30, 0, v9, vcc\n"
    "v_cmp_eq_u32 vcc, 1, v8\n"
    "s_nop 1\n"
    "v_cndmask_b32 v31, 0, v9, vcc\n"
    "v_cmp_eq_u32 vcc, 2, v8\n"
    "s_nop 1\n"
    "v_cndmask_b32 v32, 0, v9, vcc\n"
    "v_cmp_eq_u32 vcc, 3, v8\n"
    "s_nop 1\n"
    "v_cndmask_b32 v33, 0, v9, vcc\n"
    "v_cmp_eq_u32 vcc, -8, v8\n"
    "buffer_load_dwordx4 v18, s[44:47], s71 offen offset:2048 lds\n"
    "s_nop 0\n"
    "v_cndmask_b32 v242, 0, v9, vcc\n"
    "v_cmp_eq_u32 vcc, -7, v8\n"
    "s_nop 1\n"
    "v_cndmask_b32 v243, 0, v9, vcc\n"
    "v_cmp_eq_u32 vcc, -6, v8\n"
    "s_nop 1\n"
    "v_cndmask_b32 v244, 0, v9, vcc\n"
    "v_cmp_eq_u32 vcc, -5, v8\n"
    "s_nop 1\n"
    "v_cndmask_b32_e64 v245, 0, v9, vcc\n"
    "v_mfma_f32_16x16x32_bf16 v[12:15], v[230:233], v[30:33], 0\n"
    "buffer_load_dwordx4 v19, s[44:47], s71 offen offset:3072 lds\n"
    "v_mfma_f32_16x16x32_bf16 v[42:45], v[230:233], v[242:245], 0\n"
    "v_mfma_f32_16x16x32_bf16 v[46:49], v[234:237], v[30:33], 0\n"
    "v_mfma_f32_16x16x32_bf16 v[50:53], v[234:237], v[242:245], 0\n"
    "v_add_f32 v26, v26, v25\n"
    "s_nop 2\n"
    "v_pk_fma_f32 v[12:13], v[12:13], v[26:27], v[26:27] op_sel:[0,1,0] op_sel_hi:[1,1,0]\n"
    "v_pk_fma_f32 v[14:15], v[14:15], v[26:27], v[26:27] op_sel:[0,1,0] op_sel_hi:[1,1,0]\n"
    "v_pk_fma_f32 v[42:43], v[42:43], v[26:27], v[26:27] op_sel:[0,1,0] op_sel_hi:[1,1,0]\n"
    "v_pk_fma_f32 v[44:45], v[44:45], v[26:27], v[26:27] op_sel:[0,1,0] op_sel_hi:[1,1,0]\n"
    "s_add_u32 m0, s62, 0x1000\n"
    "s_add_u32 s71, s64, 0xe0000\n"
    "s_add_u32 s72, s64, 0xfff20000\n"
    "s_min_u32 s71, s71, s72\n"
    "s_nop 0\n"
    "buffer_load_dwordx4 v16, s[44:47], s71 offen offset:0 lds\n"
    "v_pk_fma_f32 v[46:47], v[46:47], v[26:27], v[26:27] op_sel:[0,1,0] op_sel_hi:[1,1,0]\n"
    "v_pk_fma_f32 v[48:49], v[48:49], v[26:27], v[26:27] op_sel:[0,1,0] op_sel_hi:[1,1,0]\n"
    "v_pk_fma_f32 v[50:51], v[50:51], v[26:27], v[26:27] op_sel:[0,1,0] op_sel_hi:[1,1,0]\n"
    "v_pk_fma_f32 v[52:53], v[52:53], v[26:27], v[26:27] op_sel:[0,1,0] op_sel_hi:[1,1,0]\n"
    "s_lshr_b32 s84, s64, 5\n"
    "s_mov_b32 s4, s84\n"
    "s_add_u32 s5, s84, 0x2000\n"
    "s_add_u32 s55, s84, 0xffff4000\n"
    "buffer_load_dwordx4 v17, s[44:47], s71 offen offset:1024 lds\n"
    "s_min_u32 s5, s5, s55\n"
    "s_add_u32 s6, s84, 0x4000\n"
    "s_add_u32 s55, s84, 0xffff6000\n"
    "s_min_u32 s6, s6, s55\n"
    "s_add_u32 s7, s84, 0x6000\n"
    "s_add_u32 s55, s84, 0xffff8000\n"
    "s_min_u32 s7, s7, s55\n"
    "s_add_u32 s8, s84, 0x8000\n"
    "buffer_load_dwordx4 v18, s[44:47], s71 offen offset:2048 lds\n"
    "s_add_u32 s55, s84, 0xffffa000\n"
    "s_min_u32 s8, s8, s55\n"
    "s_add_u32 s9, s84, 0xa000\n"
    "s_add_u32 s55, s84, 0xffffc000\n"
    "s_min_u32 s9, s9, s55\n"
    "s_add_u32 s10, s84, 0xc000\n"
    "s_add_u32 s55, s84, 0xffffe000\n"
    "s_min_u32 s10, s10, s55\n"
    "buffer_load_dwordx4 v19, s[44:47], s71 offen offset:3072 lds\n"
    "s_add_u32 s11, s84, 0x1000\n"
    "s_add_u32 s55, s84, 0xffff3000\n"
    "s_min_u32 s11, s11, s55\n"
    "s_add_u32 s12, s84, 0x3000\n"
    "s_add_u32 s55, s84, 0xffff5000\n"
    "s_min_u32 s12, s12, s55\n"
    "s_add_u32 s13, s84, 0x5000\n"
    "s_add_u32 s55, s84, 0xffff7000\n"
    "s_add_u32 m0, s62, 0x6000\n"
    "s_add_u32 s71, s64, 0x120000\n"
    "s_add_u32 s72, s64, 0xfff60000\n"
    "s_min_u32 s71, s71, s72\n"
    "buffer_load_dwordx4 v16, s[44:47], s71 offen offset:0 lds\n"
    "s_min_u32 s13, s13, s55\n"
    "s_add_u32 s14, s84, 0x7000\n"
    "s_add_u32 s55, s84, 0xffff9000\n"
    "s_min_u32 s14, s14, s55\n"
    "s_add_u32 s15, s84, 0x9000\n"
    "s_add_u32 s55, s84, 0xffffb000\n"
    "s_min_u32 s15, s15, s55\n"
    "s_add_u32 s16, s84, 0xb000\n"
    "buffer_load_dwordx4 v17, s[44:47], s71 offen offset:1024 lds\n"
    "s_add_u32 s55, s84, 0xffffd000\n"
    "s_min_u32 s16, s16, s55\n"
    "s_add_u32 s17, s84, 0xd000\n"
    "s_add_u32 s55, s84, 0xfffff000\n"
    "s_min_u32 s17, s17, s55\n"
    "v_mov_b32 v7, v37\n"
    "buffer_load_dwordx4 v18, s[44:47], s71 offen offset:2048 lds\n"
    "buffer_load_dwordx4 v19, s[44:47], s71 offen offset:3072 lds\n"
    "s_waitcnt vmcnt(26)\n"
    "ds_read_b64_tr_b16 a[112:113], v20 offset:8192\n"
    "ds_read_b64_tr_b16 a[114:115], v20 offset:8704\n"
    "ds_read_b64_tr_b16 a[116:117], v21 offset:8192\n"
    "ds_read_b64_tr_b16 a[118:119], v21 offset:8704\n"
    "ds_read_b64_tr_b16 a[120:121], v22 offset:8192\n"
    "ds_read_b64_tr_b16 a[122:123], v22 offset:8704\n"
    "ds_read_b64_tr_b16 a[124:125], v23 offset:8192\n"
    "ds_read_b64_tr_b16 a[126:127], v23 offset:8704\n"
    "s_waitcnt lgkmcnt(0)\n"
    "s_add_u32 m0, s62, 0x2000\n"
    "s_add_u32 s71, s64, 0x160000\n"
    "s_add_u32 s72, s64, 0xfffa0000\n"
    "s_min_u32 s71, s71, s72\n"
    "buffer_load_dwordx4 v16, s[44:47], s71 offen offset:0 lds\n"
    "buffer_load_dwordx4 v17, s[44:47], s71 offen offset:1024 lds\n"
    "buffer_load_dwordx4 v18, s[44:47], s71 offen offset:2048 lds\n"
    "buffer_load_dwordx4 v19, s[44:47], s71 offen offset:3072 lds\n"
    "s_waitcnt vmcnt(26)\n"
    "s_nop 0\n"
    "ds_read_b64_tr_b16 a[80:81], v20 offset:12288\n"
    "ds_read_b64_tr_b16 a[82:83], v20 offset:12800\n"
    "ds_read_b64_tr_b16 a[84:85], v21 offset:12288\n"
    "ds_read_b64_tr_b16 a[86:87], v21 offset:12800\n"
    "ds_read_b64_tr_b16 a[88:89], v22 offset:12288\n"
    "ds_read_b64_tr_b16 a[90:91], v22 offset:12800\n"
    "ds_read_b64_tr_b16 a[92:93], v23 offset:12288\n"
    "ds_read_b64_tr_b16 a[94:95], v23 offset:12800\n"
    "s_waitcnt lgkmcnt(0)\n"
    "s_add_u32 m0, s62, 0x3000\n"
    "s_add_u32 s71, s64, 0x1a0000\n"
    "s_add_u32 s72, s64, 0xfffe0000\n"
    "s_min_u32 s71, s71, s72\n"
    "buffer_load_dwordx4 v16, s[44:47], s71 offen offset:0 lds\n"
    "buffer_load_dwordx4 v17, s[44:47], s71 offen offset:1024 lds\n"
    "buffer_load_dwordx4 v18, s[44:47], s71 offen offset:2048 lds\n"
    "buffer_load_dwordx4 v19, s[44:47], s71 offen offset:3072 lds\n"
    ".Lk_join:\n"
    "s_waitcnt vmcnt(20)\n"
    "v_cmp_ne_u64_e64 vcc, s[88:89], v[238:239]\n"
    "s_nop 0\n"
    "s_cbranch_vccnz .Lk_slow_61\n"
    ".Lk_back_62:\n"
    "s_barrier\n"
    "; ---- second GEMM\n"
    "buffer_load_dwordx4 v[102:105], v7, s[36:39], s4 offen offset:0 sc1\n"
    "s_nop 5\n"
    "buffer_load_dwordx4 v[106:109], v7, s[36:39], s4 offen offset:256 sc1\n"
    "s_nop 5\n"
    "buffer_load_dwordx4 v[110:113], v7, s[36:39], s5 offen offset:0 sc1\n"
    "s_nop 5\n"
    "buffer_load_dwordx4 v[114:117], v7, s[36:39], s5 offen offset:256 sc1\n"
    "s_nop 5\n"
    "buffer_load_dwordx4 v[118:121], v7, s[36:39], s6 offen offset:0 sc1\n"
    "s_nop 5\n"
    "buffer_load_dwordx4 v[122:125], v7, s[36:39], s6 offen offset:256 sc1\n"
    "s_nop 5\n"
    "buffer_load_dwordx4 v[126:129], v7, s[36:39], s7 offen offset:0 sc1\n"
    "s_nop 5\n"
    "buffer_load_dwordx4 v[130:133], v7, s[36:39], s7 offen offset:256 sc1\n"
    "s_nop 5\n"
    "buffer_load_dwordx4 v[134:137], v7, s[36:39], s8 offen offset:0 sc1\n"
    "s_nop 5\n"
    "buffer_load_dwordx4 v[138:141], v7, s[36:39], s8 offen offset:256 sc1\n"
    "s_nop 5\n"
    "buffer_load_dwordx4 v[142:145], v7, s[36:39], s9 offen offset:0 sc1\n"
    "s_nop 5\n"
    "buffer_load_dwordx4 v[146:149], v7, s[36:39], s9 offen offset:256 sc1\n"
    "s_nop 5\n"
    "ds_read_b64_tr_b16 a[96:97], v20 offset:0\n"
    "ds_read_b64_tr_b16 a[98:99], v20 offset:512\n"
    "ds_read_b64_tr_b16 a[100:101], v21 offset:0\n"
    "ds_read_b64_tr_b16 a[102:103], v21 offset:512\n"
    "ds_read_b64_tr_b16 a[104:105], v22 offset:0\n"
    "ds_read_b64_tr_b16 a[106:107], v22 offset:512\n"
    "ds_read_b64_tr_b16 a[108:109], v23 offset:0\n"
    "ds_read_b64_tr_b16 a[110:111], v23 offset:512\n"
    "s_waitcnt vmcnt(11)\n"
    "v_mfma_f32_16x16x32_bf16 v[54:57], v[102:105], a[4:7], 0\n"
    "buffer_load_dwordx4 v[150:153], v7, s[36:39], s10 offen offset:0 sc1\n"
    "s_waitcnt vmcnt(11)\n"
    "v_mfma_f32_16x16x32_bf16 v[58:61], v[106:109], a[4:7], 0\n"
    "v_mfma_f32_16x16x32_bf16 v[70:73], v[102:105], a[8:11], 0\n"
    "buffer_load_dwordx4 v[154:157], v7, s[36:39], s10 offen offset:256 sc1\n"
    "v_mfma_f32_16x16x32_bf16 v[74:77], v[106:109], a[8:11], 0\n"
    "ds_read_b64_tr_b16 a[128:129], v20 offset:16384\n"
    "v_mfma_f32_16x16x32_bf16 v[86:89], v[102:105], a[12:15], 0\n"
    "buffer_load_dwordx4 v[158:161], v7, s[36:39], s11 offen offset:0 sc1\n"
    "v_mfma_f32_16x16x32_bf16 v[90:93], v[106:109], a[12:15], 0\n"
    "ds_read_b64_tr_b16 a[130:131], v20 offset:16896\n"
    "v_mfma_f32_16x16x32_bf16 v[12:15], v[102:105], a[0:3], v[12:15]\n"
    "buffer_load_dwordx4 v[162:165], v7, s[36:39], s11 offen offset:256 sc1\n"
    "v_mfma_f32_16x16x32_bf16 v[42:45], v[106:109], a[0:3], v[42:45]\n"
    "ds_read_b64_tr_b16 a[132:133], v21 offset:16384\n"
    "s_waitcnt vmcnt(13)\n"
    "v_mfma_f32_16x16x32_bf16 v[54:57], v[110:113], a[20:23], v[54:57]\n"
    "buffer_load_dwordx4 v[166:169], v7, s[36:39], s12 offen offset:0 sc1\n"
    "s_waitcnt vmcnt(13)\n"
    "v_mfma_f32_16x16x32_bf16 v[58:61], v[114:117], a[20:23], v[58:61]\n"
    "v_mfma_f32_16x16x32_bf16 v[70:73], v[110:113], a[24:27], v[70:73]\n"
    "buffer_load_dwordx4 v[170:173], v7, s[36:39], s12 offen offset:256 sc1\n"
    "v_mfma_f32_16x16x32_bf16 v[74:77], v[114:117], a[24:27], v[74:77]\n"
    "ds_read_b64_tr_b16 a[134:135], v21 offset:16896\n"
    "v_mfma_f32_16x16x32_bf16 v[86:89], v[110:113], a[28:31], v[86:89]\n"
    "buffer_load_dwordx4 v[174:177], v7, s[36:39], s13 offen offset:0 sc1\n"
    "v_mfma_f32_16x16x32_bf16 v[90:93], v[114:117], a[28:31], v[90:93]\n"
    "ds_read_b64_tr_b16 a[136:137], v22 offset:16384\n"
    "v_mfma_f32_16x16x32_bf16 v[12:15], v[110:113], a[16:19], v[12:15]\n"
    "buffer_load_dwordx4 v[178:181], v7, s[36:39], s13 offen offset:256 sc1\n"
    "v_mfma_f32_16x16x32_bf16 v[42:45], v[114:117], a[16:19], v[42:45]\n"
    "ds_read_b64_tr_b16 a[138:139], v22 offset:16896\n"
    "s_waitcnt vmcnt(15)\n"
    "v_mfma_f32_16x16x32_bf16 v[54:57], v[118:121], a[36:39], v[54:57]\n"
    "buffer_load_dwordx4 v[182:185], v7, s[36:39], s14 offen offset:0 sc1\n"
    "s_waitcnt vmcnt(15)\n"
    "v_mfma_f32_16x16x32_bf16 v[58:61], v[122:125], a[36:39], v[58:61]\n"
    "v_mfma_f32_16x16x32_bf16 v[70:73], v[118:121], a[40:43], v[70:73]\n"
    "buffer_load_dwordx4 v[186:189], v7, s[36:39], s14 offen offset:256 sc1\n"
    "v_mfma_f32_16x16x32_bf16 v[74:77], v[122:125], a[40:43], v[74:77]\n"
    "ds_read_b64_tr_b16 a[140:141], v23 offset:16384\n"
    "v_mfma_f32_16x16x32_bf16 v[86:89], v[118:121], a[44:47], v[86:89]\n"
    "buffer_load_dwordx4 v[190:193], v7, s[36:39], s15 offen offset:0 sc1\n"
    "v_mfma_f32_16x16x32_bf16 v[90:93], v[122:125], a[44:47], v[90:93]\n"
    "ds_read_b64_tr_b16 a[142:143], v23 offset:16896\n"
    "v_mfma_f32_16x16x32_bf16 v[12:15], v[118:121], a[32:35], v[12:15]\n"
    "buffer_load_dwordx4 v[194:197], v7, s[36:39], s15 offen offset:256 sc1\n"
    "v_mfma_f32_16x16x32_bf16 v[42:45], v[122:125], a[32:35], v[42:45]\n"
    "ds_read_b64_tr_b16 a[144:145], v20 offset:20480\n"
    "s_waitcnt vmcnt(17)\n"
    "v_mfma_f32_16x16x32_bf16 v[54:57], v[126:129], a[52:55], v[54:57]\n"
    "buffer_load_dwordx4 v[198:201], v7, s[36:39], s16 offen offset:0 sc1\n"
    "s_waitcnt vmcnt(17)\n"
    "v_mfma_f32_16x16x32_bf16 v[58:61], v[130:133], a[52:55], v[58:61]\n"
    "v_mfma_f32_16x16x32_bf16 v[70:73], v[126:129], a[56:59], v[70:73]\n"
    "buffer_load_dwordx4 v[202:205], v7, s[36:39], s16 offen offset:256 sc1\n"
    "v_mfma_f32_16x16x32_bf16 v[74:77], v[130:133], a[56:59], v[74:77]\n"
    "ds_read_b64_tr_b16 a[146:147], v20 offset:20992\n"
    "v_mfma_f32_16x16x32_bf16 v[86:89], v[126:129], a[60:63], v[86:89]\n"
    "buffer_load_dwordx4 v[206:209], v7, s[36:39], s17 offen offset:0 sc1\n"
    "v_mfma_f32_16x16x32_bf16 v[90:93], v[130:133], a[60:63], v[90:93]\n"
    "ds_read_b64_tr_b16 a[148:149], v21 offset:20480\n"
    "v_mfma_f32_16x16x32_bf16 v[12:15], v[126:129], a[48:51], v[12:15]\n"
    "buffer_load_dwordx4 v[210:213], v7, s[36:39], s17 offen offset:256 sc1\n"
    "v_mfma_f32_16x16x32_bf16 v[42:45], v[130:133], a[48:51], v[42:45]\n"
    "ds_read_b64_tr_b16 a[150:151], v21 offset:20992\n"
    "s_waitcnt vmcnt(19)\n"
    "v_mfma_f32_16x16x32_bf16 v[54:57], v[134:137], a[68:71], v[54:57]\n"
    "buffer_load_dwordx4 v[214:217], v7, s[36:39], s4 offen offset:512 sc1\n"
    "s_waitcnt vmcnt(19)\n"
    "v_mfma_f32_16x16x32_bf16 v[58:61], v[138:141], a[68:71], v[58:61]\n"
    "v_mfma_f32_16x16x32_bf16 v[70:73], v[134:137], a[72:75], v[70:73]\n"
    "ds_read_b64_tr_b16 a[152:153], v22 offset:20480\n"
    "v_mfma_f32_16x16x32_bf16 v[74:77], v[138:141], a[72:75], v[74:77]\n"
    "ds_read_b64_tr_b16 a[154:155], v22 offset:20992\n"
    "v_mfma_f32_16x16x32_bf16 v[86:89], v[134:137], a[76:79], v[86:89]\n"
    "buffer_load_dwordx4 v[218:221], v7, s[36:39], s4 offen offset:768 sc1\n"
    "v_mfma_f32_16x16x32_bf16 v[90:93], v[138:141], a[76:79], v[90:93]\n"
    "ds_read_b64_tr_b16 a[156:157], v23 offset:20480\n"
    "v_mfma_f32_16x16x32_bf16 v[12:15], v[134:137], a[64:67], v[12:15]\n"
    "ds_read_b64_tr_b16 a[158:159], v23 offset:20992\n"
    "v_mfma_f32_16x16x32_bf16 v[42:45], v[138:141], a[64:67], v[42:45]\n"
    "ds_read_b64_tr_b16 a[160:161], v20 offset:4096\n"
    "s_waitcnt vmcnt(19)\n"
    "v_mfma_f32_16x16x32_bf16 v[54:57], v[142:145], a[84:87], v[54:57]\n"
    "buffer_load_dwordx4 v[222:225], v7, s[36:39], s5 offen offset:512 sc1\n"
    "s_waitcnt vmcnt(19)\n"
    "v_mfma_f32_16x16x32_bf16 v[58:61], v[146:149], a[84:87], v[58:61]\n"
    "v_mfma_f32_16x16x32_bf16 v[70:73], v[142:145], a[88:91], v[70:73]\n"
    "ds_read_b64_tr_b16 a[162:163], v20 offset:4608\n"
    "v_mfma_f32_16x16x32_bf16 v[74:77], v[146:149], a[88:91], v[74:77]\n"
    "ds_read_b64_tr_b16 a[164:165], v21 offset:4096\n"
    "v_mfma_f32_16x16x32_bf16 v[86:89], v[142:145], a[92:95], v[86:89]\n"
    "buffer_load_dwordx4 v[226:229], v7, s[36:39], s5 offen offset:768 sc1\n"
    "v_mfma_f32_16x16x32_bf16 v[90:93], v[146:149], a[92:95], v[90:93]\n"
    "ds_read_b64_tr_b16 a[166:167], v21 offset:4608\n"
    "v_mfma_f32_16x16x32_bf16 v[12:15], v[142:145], a[80:83], v[12:15]\n"
    "ds_read_b64_tr_b16 a[168:169], v22 offset:4096\n"
    "v_mfma_f32_16x16x32_bf16 v[42:45], v[146:149], a[80:83], v[42:45]\n"
    "ds_read_b64_tr_b16 a[170:171], v22 offset:4608\n"
    "s_waitcnt vmcnt(19) lgkmcnt(15)\n"
    "v_mfma_f32_16x16x32_bf16 v[54:57], v[150:153], a[100:103], v[54:57]\n"
    "buffer_load_dwordx4 v[102:105], v7, s[36:39], s6 offen offset:512 sc1\n"
    "s_waitcnt vmcnt(19)\n"
    "v_mfma_f32_16x16x32_bf16 v[58:61], v[154:157], a[100:103], v[58:61]\n"
    "v_mfma_f32_16x16x32_bf16 v[70:73], v[150:153], a[104:107], v[70:73]\n"
    "ds_read_b64_tr_b16 a[172:173], v23 offset:4096\n"
    "v_mfma_f32_16x16x32_bf16 v[74:77], v[154:157], a[104:107], v[74:77]\n"
    "ds_read_b64_tr_b16 a[174:175], v23 offset:4608\n"
    "v_mfma_f32_16x16x32_bf16 v[86:89], v[150:153], a[108:111], v[86:89]\n"
    "buffer_load_dwordx4 v[106:109], v7, s[36:39], s6 offen offset:768 sc1\n"
    "v_mfma_f32_16x16x32_bf16 v[90:93], v[154:157], a[108:111], v[90:93]\n"
    "ds_read_b64_tr_b16 a[176:177], v20 offset:24576\n"
    "v_mfma_f32_16x16x32_bf16 v[12:15], v[150:153], a[96:99], v[12:15]\n"
    "ds_read_b64_tr_b16 a[178:179], v20 offset:25088\n"
    "v_mfma_f32_16x16x32_bf16 v[42:45], v[154:157], a[96:99], v[42:45]\n"
    "ds_read_b64_tr_b16 a[180:181], v21 offset:24576\n"
    "s_waitcnt vmcnt(19)\n"
    "v_mfma_f32_16x16x32_bf16 v[54:57], v[158:161], a[116:119], v[54:57]\n"
    "buffer_load_dwordx4 v[110:113], v7, s[36:39], s7 offen offset:512 sc1\n"
    "s_waitcnt vmcnt(19)\n"
    "v_mfma_f32_16x16x32_bf16 v[58:61], v[162:165], a[116:119], v[58:61]\n"
    "v_mfma_f32_16x16x32_bf16 v[70:73], v[158:161], a[120:123], v[70:73]\n"
    "ds_read_b64_tr_b16 a[182:183], v21 offset:25088\n"
    "v_mfma_f32_16x16x32_bf16 v[74:77], v[162:165], a[120:123], v[74:77]\n"
    "ds_read_b64_tr_b16 a[184:185], v22 offset:24576\n"
    "v_mfma_f32_16x16x32_bf16 v[86:89], v[158:161], a[124:127], v[86:89]\n"
    "buffer_load_dwordx4 v[114:117], v7, s[36:39], s7 offen offset:768 sc1\n"
    "v_mfma_f32_16x16x32_bf16 v[90:93], v[162:165], a[124:127], v[90:93]\n"
    "ds_read_b64_tr_b16 a[186:187], v22 offset:25088\n"
    "v_mfma_f32_16x16x32_bf16 v[12:15], v[158:161], a[112:115], v[12:15]\n"
    "ds_read_b64_tr_b16 a[188:189], v23 offset:24576\n"
    "v_mfma_f32_16x16x32_bf16 v[42:45], v[162:165], a[112:115], v[42:45]\n"
    "ds_read_b64_tr_b16 a[190:191], v23 offset:25088\n"
    "s_waitcnt vmcnt(19)\n"
    "v_mfma_f32_16x16x32_bf16 v[54:57], v[166:169], a[132:135], v[54:57]\n"
    "buffer_load_dwordx4 v[118:121], v7, s[36:39], s8 offen offset:512 sc1\n"
    "s_waitcnt vmcnt(19)\n"
    "v_mfma_f32_16x16x32_bf16 v[58:61], v[170:173], a[132:135], v[58:61]\n"
    "v_mfma_f32_16x16x32_bf16 v[70:73], v[166:169], a[136:139], v[70:73]\n"
    "ds_read_b64_tr_b16 a[192:193], v20 offset:8192\n"
    "v_mfma_f32_16x16x32_bf16 v[74:77], v[170:173], a[136:139], v[74:77]\n"
    "ds_read_b64_tr_b16 a[194:195], v20 offset:8704\n"
    "s_waitcnt lgkmcnt(15)\n"
    "v_mfma_f32_16x16x32_bf16 v[86:89], v[166:169], a[140:143], v[86:89]\n"
    "buffer_load_dwordx4 v[122:125], v7, s[36:39], s8 offen offset:768 sc1\n"
    "v_mfma_f32_16x16x32_bf16 v[90:93], v[170:173], a[140:143], v[90:93]\n"
    "ds_read_b64_tr_b16 a[196:197], v21 offset:8192\n"
    "v_mfma_f32_16x16x32_bf16 v[12:15], v[166:169], a[128:131], v[12:15]\n"
    "ds_read_b64_tr_b16 a[198:199], v21 offset:8704\n"
    "v_mfma_f32_16x16x32_bf16 v[42:45], v[170:173], a[128:131], v[42:45]\n"
    "ds_read_b64_tr_b16 a[200:201], v22 offset:8192\n"
    "s_waitcnt vmcnt(19)\n"
    "v_mfma_f32_16x16x32_bf16 v[54:57], v[174:177], a[148:151], v[54:57]\n"
    "buffer_load_dwordx4 v[126:129], v7, s[36:39], s9 offen offset:512 sc1\n"
    "s_waitcnt vmcnt(19)\n"
    "v_mfma_f32_16x16x32_bf16 v[58:61], v[178:181], a[148:151], v[58:61]\n"
    "v_mfma_f32_16x16x32_bf16 v[70:73], v[174:177], a[152:155], v[70:73]\n"
    "ds_read_b64_tr_b16 a[202:203], v22 offset:8704\n"
    "v_mfma_f32_16x16x32_bf16 v[74:77], v[178:181], a[152:155], v[74:77]\n"
    "ds_read_b64_tr_b16 a[204:205], v23 offset:8192\n"
    "v_mfma_f32_16x16x32_bf16 v[86:89], v[174:177], a[156:159], v[86:89]\n"
    "buffer_load_dwordx4 v[130:133], v7, s[36:39], s9 offen offset:768 sc1\n"
    "v_mfma_f32_16x16x32_bf16 v[90:93], v[178:181], a[156:159], v[90:93]\n"
    "ds_read_b64_tr_b16 a[206:207], v23 offset:8704\n"
    "v_mfma_f32_16x16x32_bf16 v[12:15], v[174:177], a[144:147], v[12:15]\n"
    "ds_read_b64_tr_b16 a[208:209], v20 offset:12288\n"
    "v_mfma_f32_16x16x32_bf16 v[42:45], v[178:181], a[144:147], v[42:45]\n"
    "ds_read_b64_tr_b16 a[210:211], v20 offset:12800\n"
    "s_waitcnt vmcnt(19) lgkmcnt(15)\n"
    "v_mfma_f32_16x16x32_bf16 v[54:57], v[182:185], a[164:167], v[54:57]\n"
    "buffer_load_dwordx4 v[134:137], v7, s[36:39], s10 offen offset:512 sc1\n"
    "s_waitcnt vmcnt(19)\n"
    "v_mfma_f32_16x16x32_bf16 v[58:61], v[186:189], a[164:167], v[58:61]\n"
    "v_mfma_f32_16x16x32_bf16 v[70:73], v[182:185], a[168:171], v[70:73]\n"
    "ds_read_b64_tr_b16 a[212:213], v21 offset:12288\n"
    "v_mfma_f32_16x16x32_bf16 v[74:77], v[186:189], a[168:171], v[74:77]\n"
    "buffer_load_dwordx4 v[138:141], v7, s[36:39], s10 offen offset:768 sc1\n"
    "v_mfma_f32_16x16x32_bf16 v[86:89], v[182:185], a[172:175], v[86:89]\n"
    "ds_read_b64_tr_b16 a[214:215], v21 offset:12800\n"
    "v_mfma_f32_16x16x32_bf16 v[90:93], v[186:189], a[172:175], v[90:93]\n"
    "ds_read_b64_tr_b16 a[216:217], v22 offset:12288\n"
    "s_waitcnt vmcnt(19) lgkmcnt(15)\n"
    "v_mfma_f32_16x16x32_bf16 v[54:57], v[190:193], a[180:183], v[54:57]\n"
    "buffer_load_dwordx4 v[142:145], v7, s[36:39], s11 offen offset:512 sc1\n"
    "s_waitcnt vmcnt(19)\n"
    "v_mfma_f32_16x16x32_bf16 v[58:61], v[194:197], a[180:183], v[58:61]\n"
    "v_mfma_f32_16x16x32_bf16 v[70:73], v[190:193], a[184:187], v[70:73]\n"
    "ds_read_b64_tr_b16 a[218:219], v22 offset:12800\n"
    "v_mfma_f32_16x16x32_bf16 v[74:77], v[194:197], a[184:187], v[74:77]\n"
    "buffer_load_dwordx4 v[146:149], v7, s[36:39], s11 offen offset:768 sc1\n"
    "s_waitcnt lgkmcnt(14)\n"
    "v_mfma_f32_16x16x32_bf16 v[86:89], v[190:193], a[188:191], v[86:89]\n"
    "v_mfma_f32_16x16x32_bf16 v[90:93], v[194:197], a[188:191], v[90:93]\n"
    "ds_read_b64_tr_b16 a[220:221], v23 offset:12288\n"
    "s_waitcnt vmcnt(19) lgkmcnt(11)\n"
    "v_mfma_f32_16x16x32_bf16 v[54:57], v[198:201], a[196:199], v[54:57]\n"
    "buffer_load_dwordx4 v[150:153], v7, s[36:39], s12 offen offset:512 sc1\n"
    "s_waitcnt vmcnt(19)\n"
    "v_mfma_f32_16x16x32_bf16 v[58:61], v[202:205], a[196:199], v[58:61]\n"
    "ds_read_b64_tr_b16 a[222:223], v23 offset:12800\n"
    "s_waitcnt vmcnt(18) lgkmcnt(4)\n"
    "v_mfma_f32_16x16x32_bf16 v[54:57], v[206:209], a[212:215], v[54:57]\n"
    "s_waitcnt vmcnt(17)\n"
    "v_mfma_f32_16x16x32_bf16 v[58:61], v[210:213], a[212:215], v[58:61]\n"
    "v_mfma_f32_16x16x32_bf16 v[70:73], v[198:201], a[200:203], v[70:73]\n"
    "v_mfma_f32_16x16x32_bf16 v[74:77], v[202:205], a[200:203], v[74:77]\n"
    "buffer_load_dwordx4 v[154:157], v7, s[36:39], s12 offen offset:768 sc1\n"
    "s_waitcnt lgkmcnt(2)\n"
    "v_mfma_f32_16x16x32_bf16 v[70:73], v[206:209], a[216:219], v[70:73]\n"
    "v_mfma_f32_16x16x32_bf16 v[74:77], v[210:213], a[216:219], v[74:77]\n"
    "s_waitcnt vmcnt(14)\n"
    "v_max3_u32 v28, v215, v217, v219\n"
    "v_mfma_f32_16x16x32_bf16 v[86:89], v[198:201], a[204:207], v[86:89]\n"
    "v_max3_u32 v28, v28, v221, v223\n"
    "v_mfma_f32_16x16x32_bf16 v[90:93], v[202:205], a[204:207], v[90:93]\n"
    "v_max3_u32 v28, v28, v225, v227\n"
    "ds_write_b128 v29, v[54:57] offset:0\n"
    "s_waitcnt lgkmcnt(1)\n"
    "v_mfma_f32_16x16x32_bf16 v[86:89], v[206:209], a[220:223], v[86:89]\n"
    "buffer_load_dwordx4 v[158:161], v7, s[36:39], s13 offen offset:512 sc1\n"
    "v_max_u32_e64 v28, v28, v229\n"
    "ds_write_b128 v29, v[58:61] offset:1024\n"
    "v_mfma_f32_16x16x32_bf16 v[90:93], v[210:213], a[220:223], v[90:93]\n"
    "v_cmp_eq_u32 vcc, 0xffbfffbf, v28\n"
    "v_mfma_f32_16x16x32_bf16 v[12:15], v[182:185], a[160:163], v[12:15]\n"
    "s_cbranch_vccnz .Lk_slow_64\n"
    ".Lk_back_65:\n"
    "v_mfma_f32_16x16x32_bf16 v[42:45], v[186:189], a[160:163], v[42:45]\n"
    "ds_write_b128 v29, v[70:73] offset:4096\n"
    "v_mfma_f32_16x16x32_bf16 v[12:15], v[190:193], a[176:179], v[12:15]\n"
    "ds_write_b128 v29, v[74:77] offset:5120\n"
    "v_mfma_f32_16x16x32_bf16 v[42:45], v[194:197], a[176:179], v[42:45]\n"
    "buffer_load_dwordx4 v[162:165], v7, s[36:39], s13 offen offset:768 sc1\n"
    "v_mfma_f32_16x16x32_bf16 v[12:15], v[198:201], a[192:195], v[12:15]\n"
    "v_mfma_f32_16x16x32_bf16 v[42:45], v[202:205], a[192:195], v[42:45]\n"
    "ds_write_b128 v29, v[86:89] offset:8192\n"
    "v_mfma_f32_16x16x32_bf16 v[12:15], v[206:209], a[208:211], v[12:15]\n"
    "ds_write_b128 v29, v[90:93] offset:9216\n"
    "v_mfma_f32_16x16x32_bf16 v[42:45], v[210:213], a[208:211], v[42:45]\n"
    "; ---- epilogue of the first half, second half\n"
    "s_waitcnt lgkmcnt(0)\n"
    "s_barrier\n"
    "ds_read_b128 v[54:57], v34 offset:0\n"
    "buffer_load_dwordx4 v[166:169], v7, s[36:39], s14 offen offset:512 sc1\n"
    "ds_read_b128 v[70:73], v35 offset:4096\n"
    "ds_read_b128 v[86:89], v36 offset:8192\n"
    "ds_read_b128 v[58:61], v34 offset:1024\n"
    "ds_read_b128 v[74:77], v35 offset:5120\n"
    "ds_read_b128 v[90:93], v36 offset:9216\n"
    "s_waitcnt lgkmcnt(5)\n"
    "v_pk_add_f32 v[12:13], v[12:13], v[54:55]\n"
    "v_pk_add_f32 v[14:15], v[14:15], v[56:57]\n"
    "s_waitcnt lgkmcnt(3)\n"
    "v_pk_add_f32 v[70:71], v[70:71], v[86:87]\n"
    "v_pk_add_f32 v[72:73], v[72:73], v[88:89]\n"
    "v_pk_add_f32 v[12:13], v[12:13], v[70:71]\n"
    "v_pk_add_f32 v[14:15], v[14:15], v[72:73]\n"
    "s_waitcnt lgkmcnt(2)\n"
    "v_pk_add_f32 v[42:43], v[42:43], v[58:59]\n"
    "v_pk_add_f32 v[44:45], v[44:45], v[60:61]\n"
    "buffer_load_dwordx4 v[170:173], v7, s[36:39], s14 offen offset:768 sc1\n"
    "s_waitcnt lgkmcnt(0)\n"
    "v_pk_add_f32 v[74:75], v[74:75], v[90:91]\n"
    "v_pk_add_f32 v[76:77], v[76:77], v[92:93]\n"
    "v_pk_add_f32 v[42:43], v[42:43], v[74:75]\n"
    "v_pk_add_f32 v[44:45], v[44:45], v[76:77]\n"
    "v_cvt_pk_bf16_f32 v70, v12, v13\n"
    "v_cvt_pk_bf16_f32 v72, v42, v43\n"
    "v_cvt_pk_bf16_f32 v71, v14, v15\n"
    "v_cvt_pk_bf16_f32 v73, v44, v45\n"
    "v_permlane16_swap_b32 v70, v72\n"
    "s_nop 0\n"
    "v_permlane16_swap_b32 v71, v73\n"
    "global_store_dwordx4 v40, v[70:73], s[30:31] offset:0 sc1\n"
    "buffer_load_dwordx4 v[174:177], v7, s[36:39], s15 offen offset:512 sc1\n"
    "buffer_load_dwordx4 v[178:181], v7, s[36:39], s15 offen offset:768 sc1\n"
    "v_mfma_f32_16x16x32_bf16 v[62:65], v[214:217], a[4:7], 0\n"
    "v_mfma_f32_16x16x32_bf16 v[66:69], v[218:221], a[4:7], 0\n"
    "v_mfma_f32_16x16x32_bf16 v[78:81], v[214:217], a[8:11], 0\n"
    "v_mfma_f32_16x16x32_bf16 v[82:85], v[218:221], a[8:11], 0\n"
    "v_mfma_f32_16x16x32_bf16 v[94:97], v[214:217], a[12:15], 0\n"
    "v_mfma_f32_16x16x32_bf16 v[98:101], v[218:221], a[12:15], 0\n"
    "v_mfma_f32_16x16x32_bf16 v[46:49], v[214:217], a[0:3], v[46:49]\n"
    "v_mfma_f32_16x16x32_bf16 v[50:53], v[218:221], a[0:3], v[50:53]\n"
    "s_waitcnt vmcnt(17)\n"
    "v_max3_u32 v28, v103, v105, v107\n"
    "v_mfma_f32_16x16x32_bf16 v[62:65], v[222:225], a[20:23], v[62:65]\n"
    "v_max3_u32 v28, v28, v109, v111\n"
    "v_mfma_f32_16x16x32_bf16 v[66:69], v[226:229], a[20:23], v[66:69]\n"
    "v_max3_u32 v28, v28, v113, v115\n"
    "v_mfma_f32_16x16x32_bf16 v[78:81], v[222:225], a[24:27], v[78:81]\n"
    "v_max_u32 v28, v28, v117\n"
    "v_mfma_f32_16x16x32_bf16 v[82:85], v[226:229], a[24:27], v[82:85]\n"
    "v_cmp_eq_u32 vcc, 0xffbfffbf, v28\n"
    "v_mfma_f32_16x16x32_bf16 v[94:97], v[222:225], a[28:31], v[94:97]\n"
    "s_cbranch_vccnz .Lk_slow_67\n"
    ".Lk_back_68:\n"
    "v_mfma_f32_16x16x32_bf16 v[98:101], v[226:229], a[28:31], v[98:101]\n"
    "v_mfma_f32_16x16x32_bf16 v[46:49], v[222:225], a[16:19], v[46:49]\n"
    "v_mfma_f32_16x16x32_bf16 v[50:53], v[226:229], a[16:19], v[50:53]\n"
    "v_mfma_f32_16x16x32_bf16 v[62:65], v[102:105], a[36:39], v[62:65]\n"
    "buffer_load_dwordx4 v[182:185], v7, s[36:39], s16 offen offset:512 sc1\n"
    "v_mfma_f32_16x16x32_bf16 v[66:69], v[106:109], a[36:39], v[66:69]\n"
    "v_mfma_f32_16x16x32_bf16 v[78:81], v[102:105], a[40:43], v[78:81]\n"
    "v_mfma_f32_16x16x32_bf16 v[82:85], v[106:109], a[40:43], v[82:85]\n"
    "v_mfma_f32_16x16x32_bf16 v[94:97], v[102:105], a[44:47], v[94:97]\n"
    "buffer_load_dwordx4 v[186:189], v7, s[36:39], s16 offen offset:768 sc1\n"
    "v_mfma_f32_16x16x32_bf16 v[98:101], v[106:109], a[44:47], v[98:101]\n"
    "v_mfma_f32_16x16x32_bf16 v[46:49], v[102:105], a[32:35], v[46:49]\n"
    "v_mfma_f32_16x16x32_bf16 v[50:53], v[106:109], a[32:35], v[50:53]\n"
    "s_waitcnt vmcnt(15)\n"
    "v_max3_u32 v28, v119, v121, v123\n"
    "v_mfma_f32_16x16x32_bf16 v[62:65], v[110:113], a[52:55], v[62:65]\n"
    "buffer_load_dwordx4 v[190:193], v7, s[36:39], s17 offen offset:512 sc1\n"
    "v_max3_u32 v28, v28, v125, v127\n"
    "v_mfma_f32_16x16x32_bf16 v[66:69], v[114:117], a[52:55], v[66:69]\n"
    "v_max3_u32 v28, v28, v129, v131\n"
    "v_mfma_f32_16x16x32_bf16 v[78:81], v[110:113], a[56:59], v[78:81]\n"
    "v_max_u32 v28, v28, v133\n"
    "v_mfma_f32_16x16x32_bf16 v[82:85], v[114:117], a[56:59], v[82:85]\n"
    "v_cmp_eq_u32 vcc, 0xffbfffbf, v28\n"
    "v_mfma_f32_16x16x32_bf16 v[94:97], v[110:113], a[60:63], v[94:97]\n"
    "buffer_load_dwordx4 v[194:197], v7, s[36:39], s17 offen offset:768 sc1\n"
    "v_mfma_f32_16x16x32_bf16 v[98:101], v[114:117], a[60:63], v[98:101]\n"
    "s_cbranch_vccnz .Lk_slow_70\n"
    ".Lk_back_71:\n"
    "v_mfma_f32_16x16x32_bf16 v[46:49], v[110:113], a[48:51], v[46:49]\n"
    "v_mfma_f32_16x16x32_bf16 v[50:53], v[114:117], a[48:51], v[50:53]\n"
    "v_mfma_f32_16x16x32_bf16 v[62:65], v[118:121], a[68:71], v[62:65]\n"
    "v_mfma_f32_16x16x32_bf16 v[66:69], v[122:125], a[68:71], v[66:69]\n"
    "v_mfma_f32_16x16x32_bf16 v[78:81], v[118:121], a[72:75], v[78:81]\n"
    "v_mfma_f32_16x16x32_bf16 v[82:85], v[122:125], a[72:75], v[82:85]\n"
    "v_mfma_f32_16x16x32_bf16 v[94:97], v[118:121], a[76:79], v[94:97]\n"
    "v_mfma_f32_16x16x32_bf16 v[98:101], v[122:125], a[76:79], v[98:101]\n"
    "v_mfma_f32_16x16x32_bf16 v[46:49], v[118:121], a[64:67], v[46:49]\n"
    "v_mfma_f32_16x16x32_bf16 v[50:53], v[122:125], a[64:67], v[50:53]\n"
    "s_waitcnt vmcnt(13)\n"
    "v_max3_u32 v28, v135, v137, v139\n"
    "v_mfma_f32_16x16x32_bf16 v[62:65], v[126:129], a[84:87], v[62:65]\n"
    "v_max3_u32 v28, v28, v141, v143\n"
    "v_mfma_f32_16x16x32_bf16 v[66:69], v[130:133], a[84:87], v[66:69]\n"
    "v_max3_u32 v28, v28, v145, v147\n"
    "v_mfma_f32_16x16x32_bf16 v[78:81], v[126:129], a[88:91], v[78:81]\n"
    "v_max_u32 v28, v28, v149\n"
    "v_mfma_f32_16x16x32_bf16 v[82:85], v[130:133], a[88:91], v[82:85]\n"
    "v_cmp_eq_u32 vcc, 0xffbfffbf, v28\n"
    "v_mfma_f32_16x16x32_bf16 v[94:97], v[126:129], a[92:95], v[94:97]\n"
    "s_cbranch_vccnz .Lk_slow_73\n"
    ".Lk_back_74:\n"
    "v_mfma_f32_16x16x32_bf16 v[98:101], v[130:133], a[92:95], v[98:101]\n"
    "v_mfma_f32_16x16x32_bf16 v[46:49], v[126:129], a[80:83], v[46:49]\n"
    "v_mfma_f32_16x16x32_bf16 v[50:53], v[130:133], a[80:83], v[50:53]\n"
    "v_mfma_f32_16x16x32_bf16 v[62:65], v[134:137], a[100:103], v[62:65]\n"
    "v_mfma_f32_16x16x32_bf16 v[66:69], v[138:141], a[100:103], v[66:69]\n"
    "v_mfma_f32_16x16x32_bf16 v[78:81], v[134:137], a[104:107], v[78:81]\n"
    "v_mfma_f32_16x16x32_bf16 v[82:85], v[138:141], a[104:107], v[82:85]\n"
    "v_mfma_f32_16x16x32_bf16 v[94:97], v[134:137], a[108:111], v[94:97]\n"
    "v_mfma_f32_16x16x32_bf16 v[98:101], v[138:141], a[108:111], v[98:101]\n"
    "v_mfma_f32_16x16x32_bf16 v[46:49], v[134:137], a[96:99], v[46:49]\n"
    "v_mfma_f32_16x16x32_bf16 v[50:53], v[138:141], a[96:99], v[50:53]\n"
    "s_waitcnt vmcnt(9)\n"
    "v_max3_u32 v28, v151, v153, v155\n"
    "v_mfma_f32_16x16x32_bf16 v[62:65], v[142:145], a[116:119], v[62:65]\n"
    "v_max3_u32 v28, v28, v157, v159\n"
    "v_mfma_f32_16x16x32_bf16 v[66:69], v[146:149], a[116:119], v[66:69]\n"
    "v_max3_u32 v28, v28, v161, v163\n"
    "v_mfma_f32_16x16x32_bf16 v[78:81], v[142:145], a[120:123], v[78:81]\n"
    "v_max_u32_e64 v28, v28, v165\n"
    "v_mfma_f32_16x16x32_bf16 v[82:85], v[146:149], a[120:123], v[82:85]\n"
    "v_cmp_eq_u32 vcc, 0xffbfffbf, v28\n"
    "v_mfma_f32_16x16x32_bf16 v[94:97], v[142:145], a[124:127], v[94:97]\n"
    "s_cbranch_vccnz .Lk_slow_76\n"
    ".Lk_back_77:\n"
    "v_mfma_f32_16x16x32_bf16 v[98:101], v[146:149], a[124:127], v[98:101]\n"
    "v_mfma_f32_16x16x32_bf16 v[46:49], v[142:145], a[112:115], v[46:49]\n"
    "v_mfma_f32_16x16x32_bf16 v[50:53], v[146:149], a[112:115], v[50:53]\n"
    "v_mfma_f32_16x16x32_bf16 v[62:65], v[150:153], a[132:135], v[62:65]\n"
    "v_mfma_f32_16x16x32_bf16 v[66:69], v[154:157], a[132:135], v[66:69]\n"
    "v_mfma_f32_16x16x32_bf16 v[78:81], v[150:153], a[136:139], v[78:81]\n"
    "v_mfma_f32_16x16x32_bf16 v[82:85], v[154:157], a[136:139], v[82:85]\n"
    "v_mfma_f32_16x16x32_bf16 v[94:97], v[150:153], a[140:143], v[94:97]\n"
    "v_mfma_f32_16x16x32_bf16 v[98:101], v[154:157], a[140:143], v[98:101]\n"
    "v_mfma_f32_16x16x32_bf16 v[46:49], v[150:153], a[128:131], v[46:49]\n"
    "v_mfma_f32_16x16x32_bf16 v[50:53], v[154:157], a[128:131], v[50:53]\n"
    "s_waitcnt vmcnt(4)\n"
    "v_max3_u32 v28, v167, v169, v171\n"
    "v_mfma_f32_16x16x32_bf16 v[62:65], v[158:161], a[148:151], v[62:65]\n"
    "v_max3_u32 v28, v28, v173, v175\n"
    "v_mfma_f32_16x16x32_bf16 v[66:69], v[162:165], a[148:151], v[66:69]\n"
    "v_max3_u32 v28, v28, v177, v179\n"
    "v_mfma_f32_16x16x32_bf16 v[78:81], v[158:161], a[152:155], v[78:81]\n"
    "v_max_u32 v28, v28, v181\n"
    "v_mfma_f32_16x16x32_bf16 v[82:85], v[162:165], a[152:155], v[82:85]\n"
    "v_cmp_eq_u32 vcc, 0xffbfffbf, v28\n"
    "v_mfma_f32_16x16x32_bf16 v[94:97], v[158:161], a[156:159], v[94:97]\n"
    "s_cbranch_vccnz .Lk_slow_79\n"
    ".Lk_back_80:\n"
    "v_mfma_f32_16x16x32_bf16 v[98:101], v[162:165], a[156:159], v[98:101]\n"
    "v_mfma_f32_16x16x32_bf16 v[46:49], v[158:161], a[144:147], v[46:49]\n"
    "v_mfma_f32_16x16x32_bf16 v[50:53], v[162:165], a[144:147], v[50:53]\n"
    "v_mfma_f32_16x16x32_bf16 v[62:65], v[166:169], a[164:167], v[62:65]\n"
    "v_mfma_f32_16x16x32_bf16 v[66:69], v[170:173], a[164:167], v[66:69]\n"
    "v_mfma_f32_16x16x32_bf16 v[78:81], v[166:169], a[168:171], v[78:81]\n"
    "v_mfma_f32_16x16x32_bf16 v[82:85], v[170:173], a[168:171], v[82:85]\n"
    "s_waitcnt vmcnt(0)\n"
    "v_max3_u32 v28, v183, v185, v187\n"
    "v_mfma_f32_16x16x32_bf16 v[94:97], v[166:169], a[172:175], v[94:97]\n"
    "v_max3_u32 v28, v28, v189, v191\n"
    "v_mfma_f32_16x16x32_bf16 v[98:101], v[170:173], a[172:175], v[98:101]\n"
    "v_max3_u32 v28, v28, v193, v195\n"
    "v_mfma_f32_16x16x32_bf16 v[62:65], v[174:177], a[180:183], v[62:65]\n"
    "v_max_u32_e64 v28, v28, v197\n"
    "v_mfma_f32_16x16x32_bf16 v[66:69], v[178:181], a[180:183], v[66:69]\n"
    "v_cmp_eq_u32 vcc, 0xffbfffbf, v28\n"
    "v_mfma_f32_16x16x32_bf16 v[78:81], v[174:177], a[184:187], v[78:81]\n"
    "s_cbranch_vccnz .Lk_slow_82\n"
    ".Lk_back_83:\n"
    "v_mfma_f32_16x16x32_bf16 v[82:85], v[178:181], a[184:187], v[82:85]\n"
    "v_mfma_f32_16x16x32_bf16 v[94:97], v[174:177], a[188:191], v[94:97]\n"
    "v_mfma_f32_16x16x32_bf16 v[98:101], v[178:181], a[188:191], v[98:101]\n"
    "v_mfma_f32_16x16x32_bf16 v[62:65], v[182:185], a[196:199], v[62:65]\n"
    "v_mfma_f32_16x16x32_bf16 v[66:69], v[186:189], a[196:199], v[66:69]\n"
    "v_mfma_f32_16x16x32_bf16 v[62:65], v[190:193], a[212:215], v[62:65]\n"
    "v_mfma_f32_16x16x32_bf16 v[66:69], v[194:197], a[212:215], v[66:69]\n"
    "v_mfma_f32_16x16x32_bf16 v[78:81], v[182:185], a[200:203], v[78:81]\n"
    "v_mfma_f32_16x16x32_bf16 v[82:85], v[186:189], a[200:203], v[82:85]\n"
    "v_mfma_f32_16x16x32_bf16 v[78:81], v[190:193], a[216:219], v[78:81]\n"
    "v_mfma_f32_16x16x32_bf16 v[82:85], v[194:197], a[216:219], v[82:85]\n"
    "v_mfma_f32_16x16x32_bf16 v[94:97], v[182:185], a[204:207], v[94:97]\n"
    "v_mfma_f32_16x16x32_bf16 v[98:101], v[186:189], a[204:207], v[98:101]\n"
    "s_nop 0\n"
    "ds_write_b128 v29, v[62:65] offset:2048\n"
    "v_mfma_f32_16x16x32_bf16 v[94:97], v[190:193], a[220:223], v[94:97]\n"
    "ds_write_b128 v29, v[66:69] offset:3072\n"
    "v_mfma_f32_16x16x32_bf16 v[98:101], v[194:197], a[220:223], v[98:101]\n"
    "v_mfma_f32_16x16x32_bf16 v[46:49], v[166:169], a[160:163], v[46:49]\n"
    "v_mfma_f32_16x16x32_bf16 v[50:53], v[170:173], a[160:163], v[50:53]\n"
    "ds_write_b128 v29, v[78:81] offset:6144\n"
    "v_mfma_f32_16x16x32_bf16 v[46:49], v[174:177], a[176:179], v[46:49]\n"
    "ds_write_b128 v29, v[82:85] offset:7168\n"
    "v_mfma_f32_16x16x32_bf16 v[50:53], v[178:181], a[176:179], v[50:53]\n"
    "v_mfma_f32_16x16x32_bf16 v[46:49], v[182:185], a[192:195], v[46:49]\n"
    "v_mfma_f32_16x16x32_bf16 v[50:53], v[186:189], a[192:195], v[50:53]\n"
    "ds_write_b128 v29, v[94:97] offset:10240\n"
    "v_mfma_f32_16x16x32_bf16 v[46:49], v[190:193], a[208:211], v[46:49]\n"
    "ds_write_b128 v29, v[98:101] offset:11264\n"
    "v_mfma_f32_16x16x32_bf16 v[50:53], v[194:197], a[208:211], v[50:53]\n"
    "; ---- epilogue of the second half\n"
    "s_waitcnt lgkmcnt(0)\n"
    "s_barrier\n"
    "ds_read_b128 v[62:65], v34 offset:2048\n"
    "ds_read_b128 v[78:81], v35 offset:6144\n"
    "ds_read_b128 v[94:97], v36 offset:10240\n"
    "ds_read_b128 v[66:69], v34 offset:3072\n"
    "ds_read_b128 v[82:85], v35 offset:7168\n"
    "ds_read_b128 v[98:101], v36 offset:11264\n"
    "s_waitcnt lgkmcnt(5)\n"
    "v_pk_add_f32 v[46:47], v[46:47], v[62:63]\n"
    "v_pk_add_f32 v[48:49], v[48:49], v[64:65]\n"
    "s_waitcnt lgkmcnt(3)\n"
    "v_pk_add_f32 v[78:79], v[78:79], v[94:95]\n"
    "v_pk_add_f32 v[80:81], v[80:81], v[96:97]\n"
    "v_pk_add_f32 v[46:47], v[46:47], v[78:79]\n"
    "v_pk_add_f32 v[48:49], v[48:49], v[80:81]\n"
    "s_waitcnt lgkmcnt(2)\n"
    "v_pk_add_f32 v[50:51], v[50:51], v[66:67]\n"
    "v_pk_add_f32 v[52:53], v[52:53], v[68:69]\n"
    "s_waitcnt lgkmcnt(0)\n"
    "v_pk_add_f32 v[82:83], v[82:83], v[98:99]\n"
    "v_pk_add_f32 v[84:85], v[84:85], v[100:101]\n"
    "v_pk_add_f32 v[50:51], v[50:51], v[82:83]\n"
    "v_pk_add_f32 v[52:53], v[52:53], v[84:85]\n"
    "v_cvt_pk_bf16_f32 v78, v46, v47\n"
    "v_cvt_pk_bf16_f32 v80, v50, v51\n"
    "v_cvt_pk_bf16_f32 v79, v48, v49\n"
    "v_cvt_pk_bf16_f32 v81, v52, v53\n"
    "v_permlane16_swap_b32 v78, v80\n"
    "s_nop 0\n"
    "v_permlane16_swap_b32 v79, v81\n"
    "global_store_dwordx4 v40, v[78:81], s[30:31] offset:64 sc1\n"
    "s_endpgm\n"
    "; ---- workgroups without a first-GEMM tile; then the out-of-line re-read loops\n"
    ".Lk_idle:\n"
    "s_waitcnt vmcnt(0) lgkmcnt(0)\n"
    "ds_read_b64_tr_b16 a[0:1], v20 offset:0\n"
    "ds_read_b64_tr_b16 a[2:3], v20 offset:512\n"
    "ds_read_b64_tr_b16 a[4:5], v21 offset:0\n"
    "ds_read_b64_tr_b16 a[6:7], v21 offset:512\n"
    "ds_read_b64_tr_b16 a[8:9], v22 offset:0\n"
    "ds_read_b64_tr_b16 a[10:11], v22 offset:512\n"
    "ds_read_b64_tr_b16 a[12:13], v23 offset:0\n"
    "ds_read_b64_tr_b16 a[14:15], v23 offset:512\n"
    "ds_read_b64_tr_b16 a[16:17], v20 offset:16384\n"
    "ds_read_b64_tr_b16 a[18:19], v20 offset:16896\n"
    "ds_read_b64_tr_b16 a[20:21], v21 offset:16384\n"
    "ds_read_b64_tr_b16 a[22:23], v21 offset:16896\n"
    "ds_read_b64_tr_b16 a[24:25], v22 offset:16384\n"
    "ds_read_b64_tr_b16 a[26:27], v22 offset:16896\n"
    "ds_read_b64_tr_b16 a[28:29], v23 offset:16384\n"
    "ds_read_b64_tr_b16 a[30:31], v23 offset:16896\n"
    "ds_read_b64_tr_b16 a[32:33], v20 offset:20480\n"
    "ds_read_b64_tr_b16 a[34:35], v20 offset:20992\n"
    "ds_read_b64_tr_b16 a[36:37], v21 offset:20480\n"
    "ds_read_b64_tr_b16 a[38:39], v21 offset:20992\n"
    "ds_read_b64_tr_b16 a[40:41], v22 offset:20480\n"
    "ds_read_b64_tr_b16 a[42:43], v22 offset:20992\n"
    "ds_read_b64_tr_b16 a[44:45], v23 offset:20480\n"
    "ds_read_b64_tr_b16 a[46:47], v23 offset:20992\n"
    "ds_read_b64_tr_b16 a[48:49], v20 offset:4096\n"
    "ds_read_b64_tr_b16 a[50:51], v20 offset:4608\n"
    "ds_read_b64_tr_b16 a[52:53], v21 offset:4096\n"
    "ds_read_b64_tr_b16 a[54:55], v21 offset:4608\n"
    "ds_read_b64_tr_b16 a[56:57], v22 offset:4096\n"
    "ds_read_b64_tr_b16 a[58:59], v22 offset:4608\n"
    "ds_read_b64_tr_b16 a[60:61], v23 offset:4096\n"
    "ds_read_b64_tr_b16 a[62:63], v23 offset:4608\n"
    "ds_read_b64_tr_b16 a[64:65], v20 offset:24576\n"
    "ds_read_b64_tr_b16 a[66:67], v20 offset:25088\n"
    "ds_read_b64_tr_b16 a[68:69], v21 offset:24576\n"
    "ds_read_b64_tr_b16 a[70:71], v21 offset:25088\n"
    "ds_read_b64_tr_b16 a[72:73], v22 offset:24576\n"
    "ds_read_b64_tr_b16 a[74:75], v22 offset:25088\n"
    "ds_read_b64_tr_b16 a[76:77], v23 offset:24576\n"
    "ds_read_b64_tr_b16 a[78:79], v23 offset:25088\n"
    "s_add_u32 m0, s62, 0x7000\n"
    "s_add_u32 s71, s64, 0x20000\n"
    "s_add_u32 s72, s64, 0xffe60000\n"
    "s_min_u32 s71, s71, s72\n"
    "buffer_load_dwordx4 v16, s[44:47], s71 offen offset:0 lds\n"
    "buffer_load_dwordx4 v17, s[44:47], s71 offen offset:1024 lds\n"
    "buffer_load_dwordx4 v18, s[44:47], s71 offen offset:2048 lds\n"
    "buffer_load_dwordx4 v19, s[44:47], s71 offen offset:3072 lds\n"
    "s_add_u32 m0, s62, 0x8000\n"
    "s_add_u32 s71, s64, 0x140000\n"
    "s_add_u32 s72, s64, 0xfff80000\n"
    "s_min_u32 s71, s71, s72\n"
    "buffer_load_dwordx4 v16, s[44:47], s71 offen offset:0 lds\n"
    "buffer_load_dwordx4 v17, s[44:47], s71 offen offset:1024 lds\n"
    "buffer_load_dwordx4 v18, s[44:47], s71 offen offset:2048 lds\n"
    "buffer_load_dwordx4 v19, s[44:47], s71 offen offset:3072 lds\n"
    "s_waitcnt lgkmcnt(15)\n"
    "s_add_u32 m0, s62, 0\n"
    "s_add_u32 s71, s64, 0x180000\n"
    "s_add_u32 s72, s64, 0xfffc0000\n"
    "s_min_u32 s71, s71, s72\n"
    "buffer_load_dwordx4 v16, s[44:47], s71 offen offset:0 lds\n"
    "buffer_load_dwordx4 v17, s[44:47], s71 offen offset:1024 lds\n"
    "buffer_load_dwordx4 v18, s[44:47], s71 offen offset:2048 lds\n"
    "buffer_load_dwordx4 v19, s[44:47], s71 offen offset:3072 lds\n"
    "s_add_u32 m0, s62, 0x4000\n"
    "s_add_u32 s71, s64, 0x60000\n"
    "s_add_u32 s72, s64, 0xffea0000\n"
    "s_min_u32 s71, s71, s72\n"
    "buffer_load_dwordx4 v16, s[44:47], s71 offen offset:0 lds\n"
    "buffer_load_dwordx4 v17, s[44:47], s71 offen offset:1024 lds\n"
    "buffer_load_dwordx4 v18, s[44:47], s71 offen offset:2048 lds\n"
    "buffer_load_dwordx4 v19, s[44:47], s71 offen offset:3072 lds\n"
    "s_add_u32 m0, s62, 0x5000\n"
    "s_add_u32 s71, s64, 0xa0000\n"
    "s_add_u32 s72, s64, 0xffee0000\n"
    "s_min_u32 s71, s71, s72\n"
    "buffer_load_dwordx4 v16, s[44:47], s71 offen offset:0 lds\n"
    "buffer_load_dwordx4 v17, s[44:47], s71 offen offset:1024 lds\n"
    "buffer_load_dwordx4 v18, s[44:47], s71 offen offset:2048 lds\n"
    "buffer_load_dwordx4 v19, s[44:47], s71 offen offset:3072 lds\n"
    "s_waitcnt lgkmcnt(8)\n"
    "s_add_u32 m0, s62, 0x1000\n"
    "s_add_u32 s71, s64, 0xe0000\n"
    "s_add_u32 s72, s64, 0xfff20000\n"
    "s_min_u32 s71, s71, s72\n"
    "buffer_load_dwordx4 v16, s[44:47], s71 offen offset:0 lds\n"
    "buffer_load_dwordx4 v17, s[44:47], s71 offen offset:1024 lds\n"
    "buffer_load_dwordx4 v18, s[44:47], s71 offen offset:2048 lds\n"
    "buffer_load_dwordx4 v19, s[44:47], s71 offen offset:3072 lds\n"
    "s_waitcnt lgkmcnt(0)\n"
    "s_add_u32 m0, s62, 0x6000\n"
    "s_add_u32 s71, s64, 0x120000\n"
    "s_add_u32 s72, s64, 0xfff60000\n"
    "s_min_u32 s71, s71, s72\n"
    "buffer_load_dwordx4 v16, s[44:47], s71 offen offset:0 lds\n"
    "buffer_load_dwordx4 v17, s[44:47], s71 offen offset:1024 lds\n"
    "buffer_load_dwordx4 v18, s[44:47], s71 offen offset:2048 lds\n"
    "buffer_load_dwordx4 v19, s[44:47], s71 offen offset:3072 lds\n"
    "s_add_u32 m0, s62, 0x2000\n"
    "s_add_u32 s71, s64, 0x160000\n"
    "s_add_u32 s72, s64, 0xfffa0000\n"
    "s_min_u32 s71, s71, s72\n"
    "buffer_load_dwordx4 v16, s[44:47], s71 offen offset:0 lds\n"
    "buffer_load_dwordx4 v17, s[44:47], s71 offen offset:1024 lds\n"
    "buffer_load_dwordx4 v18, s[44:47], s71 offen offset:2048 lds\n"
    "buffer_load_dwordx4 v19, s[44:47], s71 offen offset:3072 lds\n"
    "s_add_u32 m0, s62, 0x3000\n"
    "s_add_u32 s71, s64, 0x1a0000\n"
    "s_add_u32 s72, s64, 0xfffe0000\n"
    "s_min_u32 s71, s71, s72\n"
    "buffer_load_dwordx4 v16, s[44:47], s71 offen offset:0 lds\n"
    "buffer_load_dwordx4 v17, s[44:47], s71 offen offset:1024 lds\n"
    "buffer_load_dwordx4 v18, s[44:47], s71 offen offset:2048 lds\n"
    "buffer_load_dwordx4 v19, s[44:47], s71 offen offset:3072 lds\n"
    "buffer_load_dwordx4 v[230:233], v38, s[36:39], s85 offen offset:0 sc1\n"
    "buffer_load_dwordx4 v[234:237], v38, s[36:39], s85 offen offset:512 sc1\n"
    "buffer_load_dwordx2 v[26:27], v39, s[36:39], s86 offen offset:0 sc1\n"
    "s_waitcnt vmcnt(35)\n"
    "ds_read_b64_tr_b16 a[112:113], v20 offset:28672\n"
    "ds_read_b64_tr_b16 a[114:115], v20 offset:29184\n"
    "ds_read_b64_tr_b16 a[116:117], v21 offset:28672\n"
    "ds_read_b64_tr_b16 a[118:119], v21 offset:29184\n"
    "ds_read_b64_tr_b16 a[120:121], v22 offset:28672\n"
    "ds_read_b64_tr_b16 a[122:123], v22 offset:29184\n"
    "ds_read_b64_tr_b16 a[124:125], v23 offset:28672\n"
    "ds_read_b64_tr_b16 a[126:127], v23 offset:29184\n"
    "s_waitcnt vmcnt(31)\n"
    "ds_read_b64_tr_b16 a[80:81], v20 offset:32768\n"
    "ds_read_b64_tr_b16 a[82:83], v20 offset:33280\n"
    "ds_read_b64_tr_b16 a[84:85], v21 offset:32768\n"
    "ds_read_b64_tr_b16 a[86:87], v21 offset:33280\n"
    "ds_read_b64_tr_b16 a[88:89], v22 offset:32768\n"
    "ds_read_b64_tr_b16 a[90:91], v22 offset:33280\n"
    "ds_read_b64_tr_b16 a[92:93], v23 offset:32768\n"
    "ds_read_b64_tr_b16 a[94:95], v23 offset:33280\n"
    "s_waitcnt vmcnt(27)\n"
    "ds_read_b64_tr_b16 a[96:97], v20 offset:0\n"
    "ds_read_b64_tr_b16 a[98:99], v20 offset:512\n"
    "ds_read_b64_tr_b16 a[100:101], v21 offset:0\n"
    "ds_read_b64_tr_b16 a[102:103], v21 offset:512\n"
    "ds_read_b64_tr_b16 a[104:105], v22 offset:0\n"
    "ds_read_b64_tr_b16 a[106:107], v22 offset:512\n"
    "ds_read_b64_tr_b16 a[108:109], v23 offset:0\n"
    "ds_read_b64_tr_b16 a[110:111], v23 offset:512\n"
    "s_waitcnt vmcnt(23)\n"
    "ds_read_b64_tr_b16 a[128:129], v20 offset:16384\n"
    "ds_read_b64_tr_b16 a[130:131], v20 offset:16896\n"
    "ds_read_b64_tr_b16 a[132:133], v21 offset:16384\n"
    "ds_read_b64_tr_b16 a[134:135], v21 offset:16896\n"
    "ds_read_b64_tr_b16 a[136:137], v22 offset:16384\n"
    "ds_read_b64_tr_b16 a[138:139], v22 offset:16896\n"
    "ds_read_b64_tr_b16 a[140:141], v23 offset:16384\n"
    "ds_read_b64_tr_b16 a[142:143], v23 offset:16896\n"
    "s_waitcnt vmcnt(19)\n"
    "ds_read_b64_tr_b16 a[144:145], v20 offset:20480\n"
    "ds_read_b64_tr_b16 a[146:147], v20 offset:20992\n"
    "ds_read_b64_tr_b16 a[148:149], v21 offset:20480\n"
    "ds_read_b64_tr_b16 a[150:151], v21 offset:20992\n"
    "ds_read_b64_tr_b16 a[152:153], v22 offset:20480\n"
    "ds_read_b64_tr_b16 a[154:155], v22 offset:20992\n"
    "ds_read_b64_tr_b16 a[156:157], v23 offset:20480\n"
    "ds_read_b64_tr_b16 a[158:159], v23 offset:20992\n"
    "s_waitcnt vmcnt(15)\n"
    "ds_read_b64_tr_b16 a[160:161], v20 offset:4096\n"
    "ds_read_b64_tr_b16 a[162:163], v20 offset:4608\n"
    "ds_read_b64_tr_b16 a[164:165], v21 offset:4096\n"
    "ds_read_b64_tr_b16 a[166:167], v21 offset:4608\n"
    "ds_read_b64_tr_b16 a[168:169], v22 offset:4096\n"
    "ds_read_b64_tr_b16 a[170:171], v22 offset:4608\n"
    "ds_read_b64_tr_b16 a[172:173], v23 offset:4096\n"
    "ds_read_b64_tr_b16 a[174:175], v23 offset:4608\n"
    "s_waitcnt vmcnt(11)\n"
    "ds_read_b64_tr_b16 a[176:177], v20 offset:24576\n"
    "ds_read_b64_tr_b16 a[178:179], v20 offset:25088\n"
    "ds_read_b64_tr_b16 a[180:181], v21 offset:24576\n"
    "ds_read_b64_tr_b16 a[182:183], v21 offset:25088\n"
    "ds_read_b64_tr_b16 a[184:185], v22 offset:24576\n"
    "ds_read_b64_tr_b16 a[186:187], v22 offset:25088\n"
    "ds_read_b64_tr_b16 a[188:189], v23 offset:24576\n"
    "ds_read_b64_tr_b16 a[190:191], v23 offset:25088\n"
    "s_waitcnt vmcnt(7)\n"
    "ds_read_b64_tr_b16 a[192:193], v20 offset:8192\n"
    "ds_read_b64_tr_b16 a[194:195], v20 offset:8704\n"
    "ds_read_b64_tr_b16 a[196:197], v21 offset:8192\n"
    "ds_read_b64_tr_b16 a[198:199], v21 offset:8704\n"
    "ds_read_b64_tr_b16 a[200:201], v22 offset:8192\n"
    "ds_read_b64_tr_b16 a[202:203], v22 offset:8704\n"
    "ds_read_b64_tr_b16 a[204:205], v23 offset:8192\n"
    "ds_read_b64_tr_b16 a[206:207], v23 offset:8704\n"
    "s_waitcnt vmcnt(3)\n"
    "ds_read_b64_tr_b16 a[208:209], v20 offset:12288\n"
    "ds_read_b64_tr_b16 a[210:211], v20 offset:12800\n"
    "ds_read_b64_tr_b16 a[212:213], v21 offset:12288\n"
    "ds_read_b64_tr_b16 a[214:215], v21 offset:12800\n"
    "ds_read_b64_tr_b16 a[216:217], v22 offset:12288\n"
    "ds_read_b64_tr_b16 a[218:219], v22 offset:12800\n"
    "ds_read_b64_tr_b16 a[220:221], v23 offset:12288\n"
    "ds_read_b64_tr_b16 a[222:223], v23 offset:12800\n"
    "s_waitcnt vmcnt(0) lgkmcnt(0)\n"
    "v_max3_u32 v8, v231, v233, v235\n"
    "v_max3_u32 v8, v8, v237, v27\n"
    "v_cmp_eq_u32 vcc, 0xffbfffbf, v8\n"
    "s_nop 0\n"
    "s_cbranch_vccnz .Lk_slow_58\n"
    ".Lk_back_59:\n"
    "v_lshlrev_b32 v8, 3, v5\n"
    "v_sub_u32 v8, v6, v8\n"
    "v_and_b32 v9, 1, v6\n"
    "v_lshlrev_b32 v9, 4, v9\n"
    "s_movk_i32 s55, 0x3f80\n"
    "v_lshlrev_b32_e64 v9, v9, s55\n"
    "v_ashrrev_i32 v8, 1, v8\n"
    "v_cmp_eq_u32 vcc, 0, v8\n"
    "s_nop 1\n"
    "v_cndmask_b32 v30, 0, v9, vcc\n"
    "v_cmp_eq_u32 vcc, 1, v8\n"
    "s_nop 1\n"
    "v_cndmask_b32 v31, 0, v9, vcc\n"
    "v_cmp_eq_u32 vcc, 2, v8\n"
    "s_nop 1\n"
    "v_cndmask_b32 v32, 0, v9, vcc\n"
    "v_cmp_eq_u32 vcc, 3, v8\n"
    "s_nop 1\n"
    "v_cndmask_b32 v33, 0, v9, vcc\n"
    "v_cmp_eq_u32 vcc, -8, v8\n"
    "s_nop 1\n"
    "v_cndmask_b32 v242, 0, v9, vcc\n"
    "v_cmp_eq_u32 vcc, -7, v8\n"
    "s_nop 1\n"
    "v_cndmask_b32 v243, 0, v9, vcc\n"
    "v_cmp_eq_u32 vcc, -6, v8\n"
    "s_nop 1\n"
    "v_cndmask_b32 v244, 0, v9, vcc\n"
    "v_cmp_eq_u32 vcc, -5, v8\n"
    "s_nop 1\n"
    "v_cndmask_b32 v245, 0, v9, vcc\n"
    "v_mfma_f32_16x16x32_bf16 v[12:15], v[230:233], v[30:33], 0\n"
    "s_nop 0\n"
    "v_mfma_f32_16x16x32_bf16 v[42:45], v[230:233], v[242:245], 0\n"
    "v_mfma_f32_16x16x32_bf16 v[46:49], v[234:237], v[30:33], 0\n"
    "v_mfma_f32_16x16x32_bf16 v[50:53], v[234:237], v[242:245], 0\n"
    "v_add_f32 v26, v26, v25\n"
    "s_nop 2\n"
    "v_pk_fma_f32 v[12:13], v[12:13], v[26:27], v[26:27] op_sel:[0,1,0] op_sel_hi:[1,1,0]\n"
    "v_pk_fma_f32 v[14:15], v[14:15], v[26:27], v[26:27] op_sel:[0,1,0] op_sel_hi:[1,1,0]\n"
    "v_pk_fma_f32 v[42:43], v[42:43], v[26:27], v[26:27] op_sel:[0,1,0] op_sel_hi:[1,1,0]\n"
    "v_pk_fma_f32 v[44:45], v[44:45], v[26:27], v[26:27] op_sel:[0,1,0] op_sel_hi:[1,1,0]\n"
    "v_pk_fma_f32 v[46:47], v[46:47], v[26:27], v[26:27] op_sel:[0,1,0] op_sel_hi:[1,1,0]\n"
    "v_pk_fma_f32 v[48:49], v[48:49], v[26:27], v[26:27] op_sel:[0,1,0] op_sel_hi:[1,1,0]\n"
    "v_pk_fma_f32 v[50:51], v[50:51], v[26:27], v[26:27] op_sel:[0,1,0] op_sel_hi:[1,1,0]\n"
    "v_pk_fma_f32 v[52:53], v[52:53], v[26:27], v[26:27] op_sel:[0,1,0] op_sel_hi:[1,1,0]\n"
    "s_lshr_b32 s84, s64, 5\n"
    "s_mov_b32 s4, s84\n"
    "s_add_u32 s5, s84, 0x2000\n"
    "s_add_u32 s55, s84, 0xffff4000\n"
    "s_min_u32 s5, s5, s55\n"
    "s_add_u32 s6, s84, 0x4000\n"
    "s_add_u32 s55, s84, 0xffff6000\n"
    "s_min_u32 s6, s6, s55\n"
    "s_add_u32 s7, s84, 0x6000\n"
    "s_add_u32 s55, s84, 0xffff8000\n"
    "s_min_u32 s7, s7, s55\n"
    "s_add_u32 s8, s84, 0x8000\n"
    "s_add_u32 s55, s84, 0xffffa000\n"
    "s_min_u32 s8, s8, s55\n"
    "s_add_u32 s9, s84, 0xa000\n"
    "s_add_u32 s55, s84, 0xffffc000\n"
    "s_min_u32 s9, s9, s55\n"
    "s_add_u32 s10, s84, 0xc000\n"
    "s_add_u32 s55, s84, 0xffffe000\n"
    "s_min_u32 s10, s10, s55\n"
    "s_add_u32 s11, s84, 0x1000\n"
    "s_add_u32 s55, s84, 0xffff3000\n"
    "s_min_u32 s11, s11, s55\n"
    "s_add_u32 s12, s84, 0x3000\n"
    "s_add_u32 s55, s84, 0xffff5000\n"
    "s_min_u32 s12, s12, s55\n"
    "s_add_u32 s13, s84, 0x5000\n"
    "s_add_u32 s55, s84, 0xffff7000\n"
    "s_min_u32 s13, s13, s55\n"
    "s_add_u32 s14, s84, 0x7000\n"
    "s_add_u32 s55, s84, 0xffff9000\n"
    "s_min_u32 s14, s14, s55\n"
    "s_add_u32 s15, s84, 0x9000\n"
    "s_add_u32 s55, s84, 0xffffb000\n"
    "s_min_u32 s15, s15, s55\n"
    "s_add_u32 s16, s84, 0xb000\n"
    "s_add_u32 s55, s84, 0xffffd000\n"
    "s_min_u32 s16, s16, s55\n"
    "s_add_u32 s17, s84, 0xd000\n"
    "s_add_u32 s55, s84, 0xfffff000\n"
    "s_min_u32 s17, s17, s55\n"
    "v_mov_b32 v7, v37\n"
    "s_mov_b64 exec, 1\n"
    ".Lk_ifl1:\n"
    "s_sleep 12\n"
    "buffer_load_dwordx2 v[238:239], v41, s[36:39], s90 offen offset:0 sc1\n"
    "s_cmp_eq_u32 s54, 0\n"
    "s_cbranch_scc1 .Lk_ipx\n"
    "s_sub_u32 s54, s54, 1\n"
    "s_waitcnt vmcnt(0)\n"
    "v_cmp_ne_u64 vcc, s[88:89], v[238:239]\n"
    "s_nop 0\n"
    "s_cbranch_vccnz .Lk_ifl1\n"
    "s_mov_b64 exec, -1\n"
    ".Lk_ifl:\n"
    "s_sleep 12\n"
    "buffer_load_dwordx2 v[238:239], v41, s[36:39], s90 offen offset:0 sc1\n"
    "s_cmp_eq_u32 s54, 0\n"
    "s_cbranch_scc1 .Lk_ipx\n"
    "s_sub_u32 s54, s54, 1\n"
    "s_waitcnt vmcnt(0)\n"
    "v_cmp_ne_u64 vcc, s[88:89], v[238:239]\n"
    "s_nop 0\n"
    "s_cbranch_vccnz .Lk_ifl\n"
    ".Lk_ipx:\n"
    "s_waitcnt vmcnt(0)\n"
    "s_mov_b64 exec, -1\n"
    "s_nop 3\n"
    "s_branch .Lk_join\n"
    ".Lk_slow_1:\n"
    ".Lk_loop_3:\n"
    "s_cmp_eq_u32 s54, 0\n"
    "s_cbranch_scc1 .Lk_loop_3_x\n"
    "s_sub_u32 s54, s54, 1\n"
    "s_wqm_b64 exec, vcc\n"
    "s_nop 7\n"
    "buffer_load_dwordx4 v[112:115], v2, s[36:39], s76 offen offset:0 sc1\n"
    "buffer_load_dwordx4 v[116:119], v2, s[36:39], s77 offen offset:0 sc1\n"
    "s_waitcnt vmcnt(0)\n"
    "v_xor_b32 v112, v72, v112\n"
    "v_xor_b32 v113, v73, v113\n"
    "v_xor_b32 v114, v74, v114\n"
    "v_xor_b32 v115, v75, v115\n"
    "v_xor_b32 v116, v72, v116\n"
    "v_xor_b32 v117, v73, v117\n"
    "v_xor_b32 v118, v74, v118\n"
    "v_xor_b32 v119, v75, v119\n"
    "v_or3_b32 v8, v112, v113, v114\n"
    "v_or3_b32 v9, v116, v117, v118\n"
    "v_or3_b32 v8, v8, v115, v119\n"
    "v_bitop3_b32 v8, v8, v9, s83 bitop3:0xa8\n"
    "v_cmp_ne_u32 vcc, 0, v8\n"
    "s_nop 4\n"
    "s_cbranch_vccnz .Lk_loop_3\n"
    ".Lk_loop_3_x:\n"
    "s_mov_b64 exec, -1\n"
    "s_nop 4\n"
    "s_branch .Lk_back_2\n"
    ".Lk_slow_4:\n"
    ".Lk_loop_6:\n"
    "s_cmp_eq_u32 s54, 0\n"
    "s_cbranch_scc1 .Lk_loop_6_x\n"
    "s_sub_u32 s54, s54, 1\n"
    "s_mov_b64 exec, vcc\n"
    "s_nop 7\n"
    "buffer_load_dwordx4 v[42:45], v2, s[36:39], s76 offen offset:0 sc1\n"
    "buffer_load_dwordx4 v[50:53], v2, s[36:39], s77 offen offset:0 sc1\n"
    "s_waitcnt vmcnt(0)\n"
    "v_max3_u32 v8, v43, v45, v51\n"
    "v_max_u32 v8, v8, v53\n"
    "v_cmp_eq_u32 vcc, 0xffbfffbf, v8\n"
    "s_nop 4\n"
    "s_cbranch_vccnz .Lk_loop_6\n"
    ".Lk_loop_6_x:\n"
    "s_mov_b64 exec, -1\n"
    "s_nop 4\n"
    "s_branch .Lk_back_5\n"
    ".Lk_slow_7:\n"
    ".Lk_loop_9:\n"
    "s_cmp_eq_u32 s54, 0\n"
    "s_cbranch_scc1 .Lk_loop_9_x\n"
    "s_sub_u32 s54, s54, 1\n"
    "s_mov_b64 exec, vcc\n"
    "s_nop 7\n"
    "buffer_load_dwordx4 v[102:105], v7, s[36:39], s4 offen offset:0 sc1\n"
    "buffer_load_dwordx4 v[106:109], v7, s[36:39], s4 offen offset:256 sc1\n"
    "buffer_load_dwordx4 v[110:113], v7, s[36:39], s5 offen offset:0 sc1\n"
    "buffer_load_dwordx4 v[114:117], v7, s[36:39], s5 offen offset:256 sc1\n"
    "s_waitcnt vmcnt(0)\n"
    "v_max3_u32 v28, v103, v105, v107\n"
    "v_max3_u32 v28, v28, v109, v111\n"
    "v_max3_u32 v28, v28, v113, v115\n"
    "v_max_u32 v28, v28, v117\n"
    "v_cmp_eq_u32 vcc, 0xffbfffbf, v28\n"
    "s_nop 4\n"
    "s_cbranch_vccnz .Lk_loop_9\n"
    ".Lk_loop_9_x:\n"
    "s_mov_b64 exec, -1\n"
    "s_nop 4\n"
    "s_branch .Lk_back_8\n"
    ".Lk_slow_10:\n"
    ".Lk_loop_12:\n"
    "s_cmp_eq_u32 s54, 0\n"
    "s_cbranch_scc1 .Lk_loop_12_x\n"
    "s_sub_u32 s54, s54, 1\n"
    "s_mov_b64 exec, vcc\n"
    "s_nop 7\n"
    "buffer_load_dwordx4 v[118:121], v7, s[36:39], s6 offen offset:0 sc1\n"
    "buffer_load_dwordx4 v[122:125], v7, s[36:39], s6 offen offset:256 sc1\n"
    "buffer_load_dwordx4 v[126:129], v7, s[36:39], s7 offen offset:0 sc1\n"
    "buffer_load_dwordx4 v[130:133], v7, s[36:39], s7 offen offset:256 sc1\n"
    "s_waitcnt vmcnt(0)\n"
    "v_max3_u32 v28, v119, v121, v123\n"
    "v_max3_u32 v28, v28, v125, v127\n"
    "v_max3_u32 v28, v28, v129, v131\n"
    "v_max_u32 v28, v28, v133\n"
    "v_cmp_eq_u32 vcc, 0xffbfffbf, v28\n"
    "s_nop 4\n"
    "s_cbranch_vccnz .Lk_loop_12\n"
    ".Lk_loop_12_x:\n"
    "s_mov_b64 exec, -1\n"
    "s_nop 4\n"
    "s_branch .Lk_back_11\n"
    ".Lk_slow_13:\n"
    ".Lk_loop_15:\n"
    "s_cmp_eq_u32 s54, 0\n"
    "s_cbranch_scc1 .Lk_loop_15_x\n"
    "s_sub_u32 s54, s54, 1\n"
    "s_mov_b64 exec, vcc\n"
    "s_nop 7\n"
    "buffer_load_dwordx4 v[134:137], v7, s[36:39], s8 offen offset:0 sc1\n"
    "buffer_load_dwordx4 v[138:141], v7, s[36:39], s8 offen offset:256 sc1\n"
    "buffer_load_dwordx4 v[142:145], v7, s[36:39], s9 offen offset:0 sc1\n"
    "buffer_load_dwordx4 v[146:149], v7, s[36:39], s9 offen offset:256 sc1\n"
    "s_waitcnt vmcnt(0)\n"
    "v_max3_u32 v28, v135, v137, v139\n"
    "v_max3_u32 v28, v28, v141, v143\n"
    "v_max3_u32 v28, v28, v145, v147\n"
    "v_max_u32 v28, v28, v149\n"
    "v_cmp_eq_u32 vcc, 0xffbfffbf, v28\n"
    "s_nop 4\n"
    "s_cbranch_vccnz .Lk_loop_15\n"
    ".Lk_loop_15_x:\n"
    "s_mov_b64 exec, -1\n"
    "s_nop 4\n"
    "s_branch .Lk_back_14\n"
    ".Lk_slow_16:\n"
    ".Lk_loop_18:\n"
    "s_cmp_eq_u32 s54, 0\n"
    "s_cbranch_scc1 .Lk_loop_18_x\n"
    "s_sub_u32 s54, s54, 1\n"
    "s_mov_b64 exec, vcc\n"
    "s_nop 7\n"
    "buffer_load_dwordx4 v[150:153], v7, s[36:39], s10 offen offset:0 sc1\n"
    "buffer_load_dwordx4 v[154:157], v7, s[36:39], s10 offen offset:256 sc1\n"
    "buffer_load_dwordx4 v[158:161], v7, s[36:39], s11 offen offset:0 sc1\n"
    "buffer_load_dwordx4 v[162:165], v7, s[36:39], s11 offen offset:256 sc1\n"
    "s_waitcnt vmcnt(0)\n"
    "v_max3_u32 v28, v151, v153, v155\n"
    "v_max3_u32 v28, v28, v157, v159\n"
    "v_max3_u32 v28, v28, v161, v163\n"
    "v_max_u32 v28, v28, v165\n"
    "v_cmp_eq_u32 vcc, 0xffbfffbf, v28\n"
    "s_nop 4\n"
    "s_cbranch_vccnz .Lk_loop_18\n"
    ".Lk_loop_18_x:\n"
    "s_mov_b64 exec, -1\n"
    "s_nop 4\n"
    "s_branch .Lk_back_17\n"
    ".Lk_slow_19:\n"
    ".Lk_loop_21:\n"
    "s_cmp_eq_u32 s54, 0\n"
    "s_cbranch_scc1 .Lk_loop_21_x\n"
    "s_sub_u32 s54, s54, 1\n"
    "s_mov_b64 exec, vcc\n"
    "s_nop 7\n"
    "buffer_load_dwordx4 v[166:169], v7, s[36:39], s12 offen offset:0 sc1\n"
    "buffer_load_dwordx4 v[170:173], v7, s[36:39], s12 offen offset:256 sc1\n"
    "buffer_load_dwordx4 v[174:177], v7, s[36:39], s13 offen offset:0 sc1\n"
    "buffer_load_dwordx4 v[178:181], v7, s[36:39], s13 offen offset:256 sc1\n"
    "s_waitcnt vmcnt(0)\n"
    "v_max3_u32 v28, v167, v169, v171\n"
    "v_max3_u32 v28, v28, v173, v175\n"
    "v_max3_u32 v28, v28, v177, v179\n"
    "v_max_u32 v28, v28, v181\n"
    "v_cmp_eq_u32 vcc, 0xffbfffbf, v28\n"
    "s_nop 4\n"
    "s_cbranch_vccnz .Lk_loop_21\n"
    ".Lk_loop_21_x:\n"
    "s_mov_b64 exec, -1\n"
    "s_nop 4\n"
    "s_branch .Lk_back_20\n"
    ".Lk_slow_22:\n"
    ".Lk_loop_24:\n"
    "s_cmp_eq_u32 s54, 0\n"
    "s_cbranch_scc1 .Lk_loop_24_x\n"
    "s_sub_u32 s54, s54, 1\n"
    "s_mov_b64 exec, vcc\n"
    "s_nop 7\n"
    "buffer_load_dwordx4 v[182:185], v7, s[36:39], s14 offen offset:0 sc1\n"
    "buffer_load_dwordx4 v[186:189], v7, s[36:39], s14 offen offset:256 sc1\n"
    "buffer_load_dwordx4 v[190:193], v7, s[36:39], s15 offen offset:0 sc1\n"
    "buffer_load_dwordx4 v[194:197], v7, s[36:39], s15 offen offset:256 sc1\n"
    "s_waitcnt vmcnt(0)\n"
    "v_max3_u32 v28, v183, v185, v187\n"
    "v_max3_u32 v28, v28, v189, v191\n"
    "v_max3_u32 v28, v28, v193, v195\n"
    "v_max_u32 v28, v28, v197\n"
    "v_cmp_eq_u32 vcc, 0xffbfffbf, v28\n"
    "s_nop 4\n"
    "s_cbranch_vccnz .Lk_loop_24\n"
    ".Lk_loop_24_x:\n"
    "s_mov_b64 exec, -1\n"
    "s_nop 4\n"
    "s_branch .Lk_back_23\n"
    ".Lk_slow_25:\n"
    ".Lk_loop_27:\n"
    "s_cmp_eq_u32 s54, 0\n"
    "s_cbranch_scc1 .Lk_loop_27_x\n"
    "s_sub_u32 s54, s54, 1\n"
    "s_mov_b64 exec, vcc\n"
    "s_nop 7\n"
    "buffer_load_dwordx4 v[198:201], v7, s[36:39], s16 offen offset:0 sc1\n"
    "buffer_load_dwordx4 v[202:205], v7, s[36:39], s16 offen offset:256 sc1\n"
    "buffer_load_dwordx4 v[206:209], v7, s[36:39], s17 offen offset:0 sc1\n"
    "buffer_load_dwordx4 v[210:213], v7, s[36:39], s17 offen offset:256 sc1\n"
    "s_waitcnt vmcnt(0)\n"
    "v_max3_u32 v28, v199, v201, v203\n"
    "v_max3_u32 v28, v28, v205, v207\n"
    "v_max3_u32 v28, v28, v209, v211\n"
    "v_max_u32 v28, v28, v213\n"
    "v_cmp_eq_u32 vcc, 0xffbfffbf, v28\n"
    "s_nop 4\n"
    "s_cbranch_vccnz .Lk_loop_27\n"
    ".Lk_loop_27_x:\n"
    "s_mov_b64 exec, -1\n"
    "s_nop 4\n"
    "s_branch .Lk_back_26\n"
    ".Lk_slow_28:\n"
    ".Lk_loop_30:\n"
    "s_cmp_eq_u32 s54, 0\n"
    "s_cbranch_scc1 .Lk_loop_30_x\n"
    "s_sub_u32 s54, s54, 1\n"
    "s_mov_b64 exec, vcc\n"
    "s_nop 7\n"
    "buffer_load_dwordx4 v[214:217], v7, s[36:39], s18 offen offset:0 sc1\n"
    "buffer_load_dwordx4 v[218:221], v7, s[36:39], s18 offen offset:256 sc1\n"
    "buffer_load_dwordx4 v[222:225], v7, s[36:39], s19 offen offset:0 sc1\n"
    "buffer_load_dwordx4 v[226:229], v7, s[36:39], s19 offen offset:256 sc1\n"
    "s_waitcnt vmcnt(0)\n"
    "v_max3_u32 v28, v215, v217, v219\n"
    "v_max3_u32 v28, v28, v221, v223\n"
    "v_max3_u32 v28, v28, v225, v227\n"
    "v_max_u32 v28, v28, v229\n"
    "v_cmp_eq_u32 vcc, 0xffbfffbf, v28\n"
    "s_nop 4\n"
    "s_cbranch_vccnz .Lk_loop_30\n"
    ".Lk_loop_30_x:\n"
    "s_mov_b64 exec, -1\n"
    "s_nop 4\n"
    "s_branch .Lk_back_29\n"
    ".Lk_slow_31:\n"
    ".Lk_loop_33:\n"
    "s_cmp_eq_u32 s54, 0\n"
    "s_cbranch_scc1 .Lk_loop_33_x\n"
    "s_sub_u32 s54, s54, 1\n"
    "s_mov_b64 exec, vcc\n"
    "s_nop 7\n"
    "buffer_load_dwordx4 v[102:105], v7, s[36:39], s4 offen offset:512 sc1\n"
    "buffer_load_dwordx4 v[106:109], v7, s[36:39], s4 offen offset:768 sc1\n"
    "buffer_load_dwordx4 v[110:113], v7, s[36:39], s5 offen offset:512 sc1\n"
    "buffer_load_dwordx4 v[114:117], v7, s[36:39], s5 offen offset:768 sc1\n"
    "s_waitcnt vmcnt(0)\n"
    "v_max3_u32 v28, v103, v105, v107\n"
    "v_max3_u32 v28, v28, v109, v111\n"
    "v_max3_u32 v28, v28, v113, v115\n"
    "v_max_u32 v28, v28, v117\n"
    "v_cmp_eq_u32 vcc, 0xffbfffbf, v28\n"
    "s_nop 4\n"
    "s_cbranch_vccnz .Lk_loop_33\n"
    ".Lk_loop_33_x:\n"
    "s_mov_b64 exec, -1\n"
    "s_nop 4\n"
    "s_branch .Lk_back_32\n"
    ".Lk_slow_34:\n"
    ".Lk_loop_36:\n"
    "s_cmp_eq_u32 s54, 0\n"
    "s_cbranch_scc1 .Lk_loop_36_x\n"
    "s_sub_u32 s54, s54, 1\n"
    "s_mov_b64 exec, vcc\n"
    "s_nop 7\n"
    "buffer_load_dwordx4 v[118:121], v7, s[36:39], s6 offen offset:512 sc1\n"
    "buffer_load_dwordx4 v[122:125], v7, s[36:39], s6 offen offset:768 sc1\n"
    "buffer_load_dwordx4 v[126:129], v7, s[36:39], s7 offen offset:512 sc1\n"
    "buffer_load_dwordx4 v[130:133], v7, s[36:39], s7 offen offset:768 sc1\n"
    "s_waitcnt vmcnt(0)\n"
    "v_max3_u32 v28, v119, v121, v123\n"
    "v_max3_u32 v28, v28, v125, v127\n"
    "v_max3_u32 v28, v28, v129, v131\n"
    "v_max_u32 v28, v28, v133\n"
    "v_cmp_eq_u32 vcc, 0xffbfffbf, v28\n"
    "s_nop 4\n"
    "s_cbranch_vccnz .Lk_loop_36\n"
    ".Lk_loop_36_x:\n"
    "s_mov_b64 exec, -1\n"
    "s_nop 4\n"
    "s_branch .Lk_back_35\n"
    ".Lk_slow_37:\n"
    ".Lk_loop_39:\n"
    "s_cmp_eq_u32 s54, 0\n"
    "s_cbranch_scc1 .Lk_loop_39_x\n"
    "s_sub_u32 s54, s54, 1\n"
    "s_mov_b64 exec, vcc\n"
    "s_nop 7\n"
    "buffer_load_dwordx4 v[134:137], v7, s[36:39], s8 offen offset:512 sc1\n"
    "buffer_load_dwordx4 v[138:141], v7, s[36:39], s8 offen offset:768 sc1\n"
    "buffer_load_dwordx4 v[142:145], v7, s[36:39], s9 offen offset:512 sc1\n"
    "buffer_load_dwordx4 v[146:149], v7, s[36:39], s9 offen offset:768 sc1\n"
    "s_waitcnt vmcnt(0)\n"
    "v_max3_u32 v28, v135, v137, v139\n"
    "v_max3_u32 v28, v28, v141, v143\n"
    "v_max3_u32 v28, v28, v145, v147\n"
    "v_max_u32 v28, v28, v149\n"
    "v_cmp_eq_u32 vcc, 0xffbfffbf, v28\n"
    "s_nop 4\n"
    "s_cbranch_vccnz .Lk_loop_39\n"
    ".Lk_loop_39_x:\n"
    "s_mov_b64 exec, -1\n"
    "s_nop 4\n"
    "s_branch .Lk_back_38\n"
    ".Lk_slow_40:\n"
    ".Lk_loop_42:\n"
    "s_cmp_eq_u32 s54, 0\n"
    "s_cbranch_scc1 .Lk_loop_42_x\n"
    "s_sub_u32 s54, s54, 1\n"
    "s_mov_b64 exec, vcc\n"
    "s_nop 7\n"
    "buffer_load_dwordx4 v[150:153], v7, s[36:39], s10 offen offset:512 sc1\n"
    "buffer_load_dwordx4 v[154:157], v7, s[36:39], s10 offen offset:768 sc1\n"
    "buffer_load_dwordx4 v[158:161], v7, s[36:39], s11 offen offset:512 sc1\n"
    "buffer_load_dwordx4 v[162:165], v7, s[36:39], s11 offen offset:768 sc1\n"
    "s_waitcnt vmcnt(0)\n"
    "v_max3_u32 v28, v151, v153, v155\n"
    "v_max3_u32 v28, v28, v157, v159\n"
    "v_max3_u32 v28, v28, v161, v163\n"
    "v_max_u32 v28, v28, v165\n"
    "v_cmp_eq_u32 vcc, 0xffbfffbf, v28\n"
    "s_nop 4\n"
    "s_cbranch_vccnz .Lk_loop_42\n"
    ".Lk_loop_42_x:\n"
    "s_mov_b64 exec, -1\n"
    "s_nop 4\n"
    "s_branch .Lk_back_41\n"
    ".Lk_slow_43:\n"
    ".Lk_loop_45:\n"
    "s_cmp_eq_u32 s54, 0\n"
    "s_cbranch_scc1 .Lk_loop_45_x\n"
    "s_sub_u32 s54, s54, 1\n"
    "s_mov_b64 exec, vcc\n"
    "s_nop 7\n"
    "buffer_load_dwordx4 v[166:169], v7, s[36:39], s12 offen offset:512 sc1\n"
    "buffer_load_dwordx4 v[170:173], v7, s[36:39], s12 offen offset:768 sc1\n"
    "buffer_load_dwordx4 v[174:177], v7, s[36:39], s13 offen offset:512 sc1\n"
    "buffer_load_dwordx4 v[178:181], v7, s[36:39], s13 offen offset:768 sc1\n"
    "s_waitcnt vmcnt(0)\n"
    "v_max3_u32 v28, v167, v169, v171\n"
    "v_max3_u32 v28, v28, v173, v175\n"
    "v_max3_u32 v28, v28, v177, v179\n"
    "v_max_u32 v28, v28, v181\n"
    "v_cmp_eq_u32 vcc, 0xffbfffbf, v28\n"
    "s_nop 4\n"
    "s_cbranch_vccnz .Lk_loop_45\n"
    ".Lk_loop_45_x:\n"
    "s_mov_b64 exec, -1\n"
    "s_nop 4\n"
    "s_branch .Lk_back_44\n"
    ".Lk_slow_46:\n"
    ".Lk_loop_48:\n"
    "s_cmp_eq_u32 s54, 0\n"
    "s_cbranch_scc1 .Lk_loop_48_x\n"
    "s_sub_u32 s54, s54, 1\n"
    "s_mov_b64 exec, vcc\n"
    "s_nop 7\n"
    "buffer_load_dwordx4 v[182:185], v7, s[36:39], s14 offen offset:512 sc1\n"
    "buffer_load_dwordx4 v[186:189], v7, s[36:39], s14 offen offset:768 sc1\n"
    "buffer_load_dwordx4 v[190:193], v7, s[36:39], s15 offen offset:512 sc1\n"
    "buffer_load_dwordx4 v[194:197], v7, s[36:39], s15 offen offset:768 sc1\n"
    "s_waitcnt vmcnt(0)\n"
    "v_max3_u32 v28, v183, v185, v187\n"
    "v_max3_u32 v28, v28, v189, v191\n"
    "v_max3_u32 v28, v28, v193, v195\n"
    "v_max_u32 v28, v28, v197\n"
    "v_cmp_eq_u32 vcc, 0xffbfffbf, v28\n"
    "s_nop 4\n"
    "s_cbranch_vccnz .Lk_loop_48\n"
    ".Lk_loop_48_x:\n"
    "s_mov_b64 exec, -1\n"
    "s_nop 4\n"
    "s_branch .Lk_back_47\n"
    ".Lk_slow_49:\n"
    ".Lk_loop_51:\n"
    "s_cmp_eq_u32 s54, 0\n"
    "s_cbranch_scc1 .Lk_loop_51_x\n"
    "s_sub_u32 s54, s54, 1\n"
    "s_mov_b64 exec, vcc\n"
    "s_nop 7\n"
    "buffer_load_dwordx4 v[198:201], v7, s[36:39], s16 offen offset:512 sc1\n"
    "buffer_load_dwordx4 v[202:205], v7, s[36:39], s16 offen offset:768 sc1\n"
    "buffer_load_dwordx4 v[206:209], v7, s[36:39], s17 offen offset:512 sc1\n"
    "buffer_load_dwordx4 v[210:213], v7, s[36:39], s17 offen offset:768 sc1\n"
    "s_waitcnt vmcnt(0)\n"
    "v_max3_u32 v28, v199, v201, v203\n"
    "v_max3_u32 v28, v28, v205, v207\n"
    "v_max3_u32 v28, v28, v209, v211\n"
    "v_max_u32 v28, v28, v213\n"
    "v_cmp_eq_u32 vcc, 0xffbfffbf, v28\n"
    "s_nop 4\n"
    "s_cbranch_vccnz .Lk_loop_51\n"
    ".Lk_loop_51_x:\n"
    "s_mov_b64 exec, -1\n"
    "s_nop 4\n"
    "s_branch .Lk_back_50\n"
    ".Lk_slow_52:\n"
    ".Lk_loop_54:\n"
    "s_cmp_eq_u32 s54, 0\n"
    "s_cbranch_scc1 .Lk_loop_54_x\n"
    "s_sub_u32 s54, s54, 1\n"
    "s_mov_b64 exec, vcc\n"
    "s_nop 7\n"
    "buffer_load_dwordx4 v[214:217], v7, s[36:39], s18 offen offset:512 sc1\n"
    "buffer_load_dwordx4 v[218:221], v7, s[36:39], s18 offen offset:768 sc1\n"
    "buffer_load_dwordx4 v[222:225], v7, s[36:39], s19 offen offset:512 sc1\n"
    "buffer_load_dwordx4 v[226:229], v7, s[36:39], s19 offen offset:768 sc1\n"
    "s_waitcnt vmcnt(0)\n"
    "v_max3_u32 v28, v215, v217, v219\n"
    "v_max3_u32 v28, v28, v221, v223\n"
    "v_max3_u32 v28, v28, v225, v227\n"
    "v_max_u32 v28, v28, v229\n"
    "v_cmp_eq_u32 vcc, 0xffbfffbf, v28\n"
    "s_nop 4\n"
    "s_cbranch_vccnz .Lk_loop_54\n"
    ".Lk_loop_54_x:\n"
    "s_mov_b64 exec, -1\n"
    "s_nop 4\n"
    "s_branch .Lk_back_53\n"
    ".Lk_slow_55:\n"
    ".Lk_loop_57:\n"
    "s_cmp_eq_u32 s54, 0\n"
    "s_cbranch_scc1 .Lk_loop_57_x\n"
    "s_sub_u32 s54, s54, 1\n"
    "s_mov_b64 exec, vcc\n"
    "s_nop 7\n"
    "buffer_load_dwordx4 v[230:233], v38, s[36:39], s85 offen offset:0 sc1\n"
    "buffer_load_dwordx4 v[234:237], v38, s[36:39], s85 offen offset:512 sc1\n"
    "buffer_load_dwordx2 v[26:27], v39, s[36:39], s86 offen offset:0 sc1\n"
    "s_waitcnt vmcnt(0)\n"
    "v_max3_u32 v8, v231, v233, v235\n"
    "v_max3_u32 v8, v8, v237, v27\n"
    "v_cmp_eq_u32 vcc, 0xffbfffbf, v8\n"
    "s_nop 4\n"
    "s_cbranch_vccnz .Lk_loop_57\n"
    ".Lk_loop_57_x:\n"
    "s_mov_b64 exec, -1\n"
    "s_nop 4\n"
    "s_branch .Lk_back_56\n"
    ".Lk_slow_58:\n"
    ".Lk_loop_60:\n"
    "s_cmp_eq_u32 s54, 0\n"
    "s_cbranch_scc1 .Lk_loop_60_x\n"
    "s_sub_u32 s54, s54, 1\n"
    "s_mov_b64 exec, vcc\n"
    "s_nop 7\n"
    "buffer_load_dwordx4 v[230:233], v38, s[36:39], s85 offen offset:0 sc1\n"
    "buffer_load_dwordx4 v[234:237], v38, s[36:39], s85 offen offset:512 sc1\n"
    "buffer_load_dwordx2 v[26:27], v39, s[36:39], s86 offen offset:0 sc1\n"
    "s_waitcnt vmcnt(0)\n"
    "v_max3_u32 v8, v231, v233, v235\n"
    "v_max3_u32 v8, v8, v237, v27\n"
    "v_cmp_eq_u32 vcc, 0xffbfffbf, v8\n"
    "s_nop 4\n"
    "s_cbranch_vccnz .Lk_loop_60\n"
    ".Lk_loop_60_x:\n"
    "s_mov_b64 exec, -1\n"
    "s_nop 4\n"
    "s_branch .Lk_back_59\n"
    ".Lk_slow_61:\n"
    ".Lk_loop_63:\n"
    "s_cmp_eq_u32 s54, 0\n"
    "s_cbranch_scc1 .Lk_loop_63_x\n"
    "s_sub_u32 s54, s54, 1\n"
    "s_mov_b64 exec, vcc\n"
    "s_nop 7\n"
    "buffer_load_dwordx2 v[238:239], v41, s[36:39], s90 offen offset:0 sc1\n"
    "s_waitcnt vmcnt(0)\n"
    "v_cmp_ne_u64 vcc, s[88:89], v[238:239]\n"
    "s_nop 4\n"
    "s_cbranch_vccnz .Lk_loop_63\n"
    ".Lk_loop_63_x:\n"
    "s_mov_b64 exec, -1\n"
    "s_nop 4\n"
    "s_branch .Lk_back_62\n"
    ".Lk_slow_64:\n"
    ".Lk_loop_66:\n"
    "s_cmp_eq_u32 s54, 0\n"
    "s_cbranch_scc1 .Lk_loop_66_x\n"
    "s_sub_u32 s54, s54, 1\n"
    "s_mov_b64 exec, vcc\n"
    "s_nop 7\n"
    "buffer_load_dwordx4 v[214:217], v7, s[36:39], s4 offen offset:512 sc1\n"
    "buffer_load_dwordx4 v[218:221], v7, s[36:39], s4 offen offset:768 sc1\n"
    "buffer_load_dwordx4 v[222:225], v7, s[36:39], s5 offen offset:512 sc1\n"
    "buffer_load_dwordx4 v[226:229], v7, s[36:39], s5 offen offset:768 sc1\n"
    "s_waitcnt vmcnt(0)\n"
    "v_max3_u32 v28, v215, v217, v219\n"
    "v_max3_u32 v28, v28, v221, v223\n"
    "v_max3_u32 v28, v28, v225, v227\n"
    "v_max_u32 v28, v28, v229\n"
    "v_cmp_eq_u32 vcc, 0xffbfffbf, v28\n"
    "s_nop 4\n"
    "s_cbranch_vccnz .Lk_loop_66\n"
    ".Lk_loop_66_x:\n"
    "s_mov_b64 exec, -1\n"
    "s_nop 4\n"
    "s_branch .Lk_back_65\n"
    ".Lk_slow_67:\n"
    ".Lk_loop_69:\n"
    "s_cmp_eq_u32 s54, 0\n"
    "s_cbranch_scc1 .Lk_loop_69_x\n"
    "s_sub_u32 s54, s54, 1\n"
    "s_mov_b64 exec, vcc\n"
    "s_nop 7\n"
    "buffer_load_dwordx4 v[102:105], v7, s[36:39], s6 offen offset:512 sc1\n"
    "buffer_load_dwordx4 v[106:109], v7, s[36:39], s6 offen offset:768 sc1\n"
    "buffer_load_dwordx4 v[110:113], v7, s[36:39], s7 offen offset:512 sc1\n"
    "buffer_load_dwordx4 v[114:117], v7, s[36:39], s7 offen offset:768 sc1\n"
    "s_waitcnt vmcnt(0)\n"
    "v_max3_u32 v28, v103, v105, v107\n"
    "v_max3_u32 v28, v28, v109, v111\n"
    "v_max3_u32 v28, v28, v113, v115\n"
    "v_max_u32 v28, v28, v117\n"
    "v_cmp_eq_u32 vcc, 0xffbfffbf, v28\n"
    "s_nop 4\n"
    "s_cbranch_vccnz .Lk_loop_69\n"
    ".Lk_loop_69_x:\n"
    "s_mov_b64 exec, -1\n"
    "s_nop 4\n"
    "s_branch .Lk_back_68\n"
    ".Lk_slow_70:\n"
    ".Lk_loop_72:\n"
    "s_cmp_eq_u32 s54, 0\n"
    "s_cbranch_scc1 .Lk_loop_72_x\n"
    "s_sub_u32 s54, s54, 1\n"
    "s_mov_b64 exec, vcc\n"
    "s_nop 7\n"
    "buffer_load_dwordx4 v[118:121], v7, s[36:39], s8 offen offset:512 sc1\n"
    "buffer_load_dwordx4 v[122:125], v7, s[36:39], s8 offen offset:768 sc1\n"
    "buffer_load_dwordx4 v[126:129], v7, s[36:39], s9 offen offset:512 sc1\n"
    "buffer_load_dwordx4 v[130:133], v7, s[36:39], s9 offen offset:768 sc1\n"
    "s_waitcnt vmcnt(0)\n"
    "v_max3_u32 v28, v119, v121, v123\n"
    "v_max3_u32 v28, v28, v125, v127\n"
    "v_max3_u32 v28, v28, v129, v131\n"
    "v_max_u32 v28, v28, v133\n"
    "v_cmp_eq_u32 vcc, 0xffbfffbf, v28\n"
    "s_nop 4\n"
    "s_cbranch_vccnz .Lk_loop_72\n"
    ".Lk_loop_72_x:\n"
    "s_mov_b64 exec, -1\n"
    "s_nop 4\n"
    "s_branch .Lk_back_71\n"
    ".Lk_slow_73:\n"
    ".Lk_loop_75:\n"
    "s_cmp_eq_u32 s54, 0\n"
    "s_cbranch_scc1 .Lk_loop_75_x\n"
    "s_sub_u32 s54, s54, 1\n"
    "s_mov_b64 exec, vcc\n"
    "s_nop 7\n"
    "buffer_load_dwordx4 v[134:137], v7, s[36:39], s10 offen offset:512 sc1\n"
    "buffer_load_dwordx4 v[138:141], v7, s[36:39], s10 offen offset:768 sc1\n"
    "buffer_load_dwordx4 v[142:145], v7, s[36:39], s11 offen offset:512 sc1\n"
    "buffer_load_dwordx4 v[146:149], v7, s[36:39], s11 offen offset:768 sc1\n"
    "s_waitcnt vmcnt(0)\n"
    "v_max3_u32 v28, v135, v137, v139\n"
    "v_max3_u32 v28, v28, v141, v143\n"
    "v_max3_u32 v28, v28, v145, v147\n"
    "v_max_u32 v28, v28, v149\n"
    "v_cmp_eq_u32 vcc, 0xffbfffbf, v28\n"
    "s_nop 4\n"
    "s_cbranch_vccnz .Lk_loop_75\n"
    ".Lk_loop_75_x:\n"
    "s_mov_b64 exec, -1\n"
    "s_nop 4\n"
    "s_branch .Lk_back_74\n"
    ".Lk_slow_76:\n"
    ".Lk_loop_78:\n"
    "s_cmp_eq_u32 s54, 0\n"
    "s_cbranch_scc1 .Lk_loop_78_x\n"
    "s_sub_u32 s54, s54, 1\n"
    "s_mov_b64 exec, vcc\n"
    "s_nop 7\n"
    "buffer_load_dwordx4 v[150:153], v7, s[36:39], s12 offen offset:512 sc1\n"
    "buffer_load_dwordx4 v[154:157], v7, s[36:39], s12 offen offset:768 sc1\n"
    "buffer_load_dwordx4 v[158:161], v7, s[36:39], s13 offen offset:512 sc1\n"
    "buffer_load_dwordx4 v[162:165], v7, s[36:39], s13 offen offset:768 sc1\n"
    "s_waitcnt vmcnt(0)\n"
    "v_max3_u32 v28, v151, v153, v155\n"
    "v_max3_u32 v28, v28, v157, v159\n"
    "v_max3_u32 v28, v28, v161, v163\n"
    "v_max_u32 v28, v28, v165\n"
    "v_cmp_eq_u32 vcc, 0xffbfffbf, v28\n"
    "s_nop 4\n"
    "s_cbranch_vccnz .Lk_loop_78\n"
    ".Lk_loop_78_x:\n"
    "s_mov_b64 exec, -1\n"
    "s_nop 4\n"
    "s_branch .Lk_back_77\n"
    ".Lk_slow_79:\n"
    ".Lk_loop_81:\n"
    "s_cmp_eq_u32 s54, 0\n"
    "s_cbranch_scc1 .Lk_loop_81_x\n"
    "s_sub_u32 s54, s54, 1\n"
    "s_mov_b64 exec, vcc\n"
    "s_nop 7\n"
    "buffer_load_dwordx4 v[166:169], v7, s[36:39], s14 offen offset:512 sc1\n"
    "buffer_load_dwordx4 v[170:173], v7, s[36:39], s14 offen offset:768 sc1\n"
    "buffer_load_dwordx4 v[174:177], v7, s[36:39], s15 offen offset:512 sc1\n"
    "buffer_load_dwordx4 v[178:181], v7, s[36:39], s15 offen offset:768 sc1\n"
    "s_waitcnt vmcnt(0)\n"
    "v_max3_u32 v28, v167, v169, v171\n"
    "v_max3_u32 v28, v28, v173, v175\n"
    "v_max3_u32 v28, v28, v177, v179\n"
    "v_max_u32 v28, v28, v181\n"
    "v_cmp_eq_u32 vcc, 0xffbfffbf, v28\n"
    "s_nop 4\n"
    "s_cbranch_vccnz .Lk_loop_81\n"
    ".Lk_loop_81_x:\n"
    "s_mov_b64 exec, -1\n"
    "s_nop 4\n"
    "s_branch .Lk_back_80\n"
    ".Lk_slow_82:\n"
    ".Lk_loop_84:\n"
    "s_cmp_eq_u32 s54, 0\n"
    "s_cbranch_scc1 .Lk_loop_84_x\n"
    "s_sub_u32 s54, s54, 1\n"
    "s_mov_b64 exec, vcc\n"
    "s_nop 7\n"
    "buffer_load_dwordx4 v[182:185], v7, s[36:39], s16 offen offset:512 sc1\n"
    "buffer_load_dwordx4 v[186:189], v7, s[36:39], s16 offen offset:768 sc1\n"
    "buffer_load_dwordx4 v[190:193], v7, s[36:39], s17 offen offset:512 sc1\n"
    "buffer_load_dwordx4 v[194:197], v7, s[36:39], s17 offen offset:768 sc1\n"
    "s_waitcnt vmcnt(0)\n"
    "v_max3_u32 v28, v183, v185, v187\n"
    "v_max3_u32 v28, v28, v189, v191\n"
    "v_max3_u32 v28, v28, v193, v195\n"
    "v_max_u32 v28, v28, v197\n"
    "v_cmp_eq_u32 vcc, 0xffbfffbf, v28\n"
    "s_nop 4\n"
    "s_cbranch_vccnz .Lk_loop_84\n"
    ".Lk_loop_84_x:\n"
    "s_mov_b64 exec, -1\n"
    "s_nop 4\n"
    "s_branch .Lk_back_83\n"
    ".Lk_end:\n"
      :
      : "{s[0:1]}"(__builtin_amdgcn_kernarg_segment_ptr()), "{s2}"(blockIdx.x), "{v0}"(threadIdx.x),
        "{s100}"((unsigned)(size_t)(__attribute__((address_space(3))) char*)lds)
      : "memory", "vcc",
      "v1", "v2", "v3", "v4", "v5", "v6", "v7", "v8", "v9", "v10", "v11", "v12", "v13", "v14", "v15", "v16",
      "v17", "v18", "v19", "v20", "v21", "v22", "v23", "v24", "v25", "v26", "v27", "v28", "v29", "v30", "v31", "v32",
      "v33", "v34", "v35", "v36", "v37", "v38", "v39", "v40", "v41", "v42", "v43", "v44", "v45", "v46", "v47", "v48",
      "v49", "v50", "v51", "v52", "v53", "v54", "v55", "v56", "v57", "v58", "v59", "v60", "v61", "v62", "v63", "v64",
      "v65", "v66", "v67", "v68", "v69", "v70", "v71", "v72", "v73", "v74", "v75", "v76", "v77", "v78", "v79", "v80",
      "v81", "v82", "v83", "v84", "v85", "v86", "v87", "v88", "v89", "v90", "v91", "v92", "v93", "v94", "v95", "v96",
      "v97", "v98", "v99", "v100", "v101", "v102", "v103", "v104", "v105", "v106", "v107", "v108", "v109", "v110", "v111", "v112",
      "v113", "v114", "v115", "v116", "v117", "v118", "v119", "v120", "v121", "v122", "v123", "v124", "v125", "v126", "v127", "v128",
      "v129", "v130", "v131", "v132", "v133", "v134", "v135", "v136", "v137", "v138", "v139", "v140", "v141", "v142", "v143", "v144",
      "v145", "v146", "v147", "v148", "v149", "v150", "v151", "v152", "v153", "v154", "v155", "v156", "v157", "v158", "v159", "v160",
      "v161", "v162", "v163", "v164", "v165", "v166", "v167", "v168", "v169", "v170", "v171", "v172", "v173", "v174", "v175", "v176",
      "v177", "v178", "v179", "v180", "v181", "v182", "v183", "v184", "v185", "v186", "v187", "v188", "v189", "v190", "v191", "v192",
      "v193", "v194", "v195", "v196", "v197", "v198", "v199", "v200", "v201", "v202", "v203", "v204", "v205", "v206", "v207", "v208",
      "v209", "v210", "v211", "v212", "v213", "v214", "v215", "v216", "v217", "v218", "v219", "v220", "v221", "v222", "v223", "v224",
      "v225", "v226", "v227", "v228", "v229", "v230", "v231", "v232", "v233", "v234", "v235", "v236", "v237", "v238", "v239", "v240",
      "v241", "v242", "v243", "v244", "v245", "v246", "v247", "v248", "v249", "v250", "v251", "v252", "v253", "v254", "v255", "a0",
      "a1", "a2", "a3", "a4", "a5", "a6", "a7", "a8", "a9", "a10", "a11", "a12", "a13", "a14", "a15", "a16",
      "a17", "a18", "a19", "a20", "a21", "a22", "a23", "a24", "a25", "a26", "a27", "a28", "a29", "a30", "a31", "a32",
      "a33", "a34", "a35", "a36", "a37", "a38", "a39", "a40", "a41", "a42", "a43", "a44", "a45", "a46", "a47", "a48",
      "a49", "a50", "a51", "a52", "a53", "a54", "a55", "a56", "a57", "a58", "a59", "a60", "a61", "a62", "a63", "a64",
      "a65", "a66", "a67", "a68", "a69", "a70", "a71", "a72", "a73", "a74", "a75", "a76", "a77", "a78", "a79", "a80",
      "a81", "a82", "a83", "a84", "a85", "a86", "a87", "a88", "a89", "a90", "a91", "a92", "a93", "a94", "a95", "a96",
      "a97", "a98", "a99", "a100", "a101", "a102", "a103", "a104", "a105", "a106", "a107", "a108", "a109", "a110", "a111", "a112",
      "a113", "a114", "a115", "a116", "a117", "a118", "a119", "a120", "a121", "a122", "a123", "a124", "a125", "a126", "a127", "a128",
      "a129", "a130", "a131", "a132", "a133", "a134", "a135", "a136", "a137", "a138", "a139", "a140", "a141", "a142", "a143", "a144",
      "a145", "a146", "a147", "a148", "a149", "a150", "a151", "a152", "a153", "a154", "a155", "a156", "a157", "a158", "a159", "a160",
      "a161", "a162", "a163", "a164", "a165", "a166", "a167", "a168", "a169", "a170", "a171", "a172", "a173", "a174", "a175", "a176",
      "a177", "a178", "a179", "a180", "a181", "a182", "a183", "a184", "a185", "a186", "a187", "a188", "a189", "a190", "a191", "a192",
      "a193", "a194", "a195", "a196", "a197", "a198", "a199", "a200", "a201", "a202", "a203", "a204", "a205", "a206", "a207", "a208",
      "a209", "a210", "a211", "a212", "a213", "a214", "a215", "a216", "a217", "a218", "a219", "a220", "a221", "a222", "a223", "a224",
      "a225", "a226", "a227", "a228", "a229", "a230", "a231", "a232", "a233", "a234", "a235", "a236", "a237", "a238", "a239", "a240",
      "a241", "a242", "a243", "a244", "a245", "a246", "a247", "a248", "a249", "a250", "a251", "a252", "a253", "a254", "a255", "s3",
      "s4", "s5", "s6", "s7", "s8", "s9", "s10", "s11", "s12", "s13", "s14", "s15", "s16", "s17", "s18", "s19",
      "s20", "s21", "s22", "s23", "s24", "s25", "s26", "s27", "s28", "s29", "s30", "s31", "s33", "s34", "s35", "s36",
      "s37", "s38", "s39", "s40", "s41", "s42", "s43", "s44", "s45", "s46", "s47", "s48", "s49", "s50", "s51", "s52",
      "s53", "s54", "s55", "s56", "s57", "s58", "s59", "s60", "s61", "s62", "s63", "s64", "s65", "s66", "s67", "s68",
      "s69", "s70", "s71", "s72", "s73", "s74", "s75", "s76", "s77", "s78", "s79", "s80", "s81", "s82", "s83", "s84",
      "s85", "s86", "s87", "s88", "s89", "s90", "s91", "s92", "s93", "s94", "s95", "s96", "s97", "s98", "s99");
#endif
}

extern "C" void kernel_launch(void* const* d_in, const int* in_sizes, int n_in, void* d_out, int out_size,
                              void* d_ws, size_t ws_size, hipStream_t stream) {
  fused_s4_mixer_kernel<<<256, 256, 0, stream>>>(d_in[0], d_in[1], d_in[2], d_in[3], d_in[4], d_in[5], d_in[6],
                                                 d_in[7], d_in[8], d_in[10], d_in[11], d_in[12], d_in[13], d_out,
                                                 d_ws, d_ws);
}
```
